```python
import torch

def _softmax(x, axis=-1):
    return torch.softmax(x, dim=axis)
def _gelu(x):
    return torch.nn.functional.gelu(x, approximate="tanh")
def _silu(x):
    return torch.nn.functional.silu(x)
def _sigmoid(x):
    return torch.sigmoid(x)
def _layernorm(x, axis=-1, eps=1e-5):
    m = x.mean(dim=axis, keepdim=True)
    v = x.var(dim=axis, keepdim=True, unbiased=False)
    return (x - m) / torch.sqrt(v + eps)
def _scatter_add(tgt, idx, src, axis=0):
    return tgt.clone().index_add_(axis, idx, src)
def _segment_sum(src, seg, n):
    out = torch.zeros((n,) + src.shape[1:], dtype=src.dtype)
    return out.index_add_(0, seg, src)
def _pad(x, pw):
    flat = tuple(v for pair in reversed(pw) for v in pair)
    return torch.nn.functional.pad(x, flat)

kernel_name = 'recurrent_scan__grouped_kv_mqa__moe_arch_mod__prompt'

def setup_inputs():

    _w = (lambda a: (lambda t: t.to(torch.bfloat16) if t.is_floating_point() else t)(torch.as_tensor(a)))
    def _A_setup():
        B, S, D, H = 1, 2048, 640, 32
        Dh = D // H
        return {
            "x": _w((lambda *s: torch.randn(*s, dtype=torch.bfloat16) * 0.05)(B, S, D)),
            "wq": _w((lambda *s: torch.randn(*s, dtype=torch.bfloat16) * 0.05)(D, H * Dh)),
            "wk": _w((lambda *s: torch.randn(*s, dtype=torch.bfloat16) * 0.05)(D, Dh)),
            "wv": _w((lambda *s: torch.randn(*s, dtype=torch.bfloat16) * 0.05)(D, Dh)),
            "wo": _w((lambda *s: torch.randn(*s, dtype=torch.bfloat16) * 0.05)(H * Dh, D)),
        }
    _A_in = _A_setup()

    def _B_setup():
        B, S, D, F, C = 1, 2048, 640, 1728, 1
        return {
            "x": _w((lambda *s: torch.randn(*s, dtype=torch.bfloat16) * 0.05)(B, S, D)),
            "w_r": _w((lambda *s: torch.randn(*s, dtype=torch.bfloat16) * 1.6)(D)),
            "w_gate": _w((lambda *s: torch.randn(*s, dtype=torch.bfloat16) * 0.1)(D, F)),
            "w_up": _w((lambda *s: torch.randn(*s, dtype=torch.bfloat16) * 0.1)(D, F)),
            "w_down": _w((lambda *s: torch.randn(*s, dtype=torch.bfloat16) * 0.1)(F, D)),
        }
    _B_in = _B_setup()

    return {
        "A_x": _A_in["x"],
        "A_wq": _A_in["wq"],
        "A_wk": _A_in["wk"],
        "A_wv": _A_in["wv"],
        "A_wo": _A_in["wo"],
        "B_x": _B_in["x"],
        "B_w_r": _B_in["w_r"],
        "B_w_gate": _B_in["w_gate"],
        "B_w_up": _B_in["w_up"],
        "B_w_down": _B_in["w_down"],
    }

def reference(A_x, A_wq, A_wk, A_wv, A_wo, B_x, B_w_r, B_w_gate, B_w_up, B_w_down):
    def _A_ref(x, wq, wk, wv, wo):
        B, S, D = x.shape
        H = 32
        Dh = D // H
        q = (lambda x, *d: x.permute(*d))((x @ wq).reshape(B, S, H, Dh), 0, 2, 1, 3)
        k = x @ wk
        v = x @ wv
        scores = torch.einsum("bhqd,bkd->bhqk", q, k) * (Dh ** -0.5)
        p = _softmax(scores, axis=-1)
        o = torch.einsum("bhqk,bkd->bhqd", p, v)
        return (lambda x, *d: x.permute(*d))(o, 0, 2, 1, 3).reshape(B, S, H * Dh) @ wo

    def _B_ref(x, w_r, w_gate, w_up, w_down):
        B, S, D = x.shape
        C = 1
        r = x @ w_r
        idx = torch.argsort(-r, axis=-1)[:, :C]
        idx_e = (lambda x, n, axis: torch.repeat_interleave(x, n, dim=axis))(idx.reshape(B, C, 1), D, axis=2)
        x_sel = (lambda x, i, axis: torch.take_along_dim(x, i, dim=axis))(x, idx_e, axis=1)
        g = _silu(x_sel @ w_gate)
        u = x_sel @ w_up
        y = (g * u) @ w_down
        r_sel = _sigmoid((lambda x, i, axis: torch.take_along_dim(x, i, dim=axis))(r, idx, axis=1))
        y = y * r_sel.reshape(B, C, 1)
        b_off = (torch.arange(B) * S).reshape(B, 1)
        flat_idx = (idx + b_off).reshape(B * C)
        zero = (x * 0.0).reshape(B * S, D)
        upd = _scatter_add(zero, flat_idx, y.reshape(B * C, D), axis=0)
        return x + upd.reshape(B, S, D)

    x = A_x
    xn = _layernorm(x, axis=-1)
    s = _A_ref(xn, A_wq, A_wk, A_wv, A_wo)
    g = _silu(_B_ref(xn, B_w_r, B_w_gate, B_w_up, B_w_down))
    return x + s * g
```

```cpp
#include <hip/hip_runtime.h>
#include <hip/hip_bf16.h>
#include <stdint.h>

typedef __attribute__((ext_vector_type(8))) __bf16 bf16x8;
typedef __attribute__((ext_vector_type(2))) __bf16 bf16x2;
typedef __attribute__((ext_vector_type(4))) short s16x4;
typedef __attribute__((ext_vector_type(8))) short s16x8;
typedef __attribute__((ext_vector_type(2))) float f32x2;
typedef __attribute__((ext_vector_type(4))) float f32x4;
typedef __attribute__((ext_vector_type(16))) float f32x16;
typedef __attribute__((ext_vector_type(2))) unsigned u32x2;
typedef __attribute__((ext_vector_type(4))) unsigned u32x4;
typedef unsigned short u16;
#define LAS __attribute__((address_space(3)))
#define DI __device__ __forceinline__

constexpr int S_ = 2048, D_ = 640, F_ = 1728;
constexpr int NFFN = 216;

constexpr size_t WS_XN = 0;
constexpr size_t WS_O = WS_XN + (size_t)S_ * D_ * 2;
constexpr size_t WS_KF1 = WS_O + (size_t)S_ * D_ * 2;
constexpr size_t WS_KF2 = WS_KF1 + 65536;
constexpr size_t WS_VF = WS_KF2 + 16384;
constexpr size_t WS_SUM = WS_VF + 81920;
constexpr size_t WS_YP = WS_SUM + 4096;

DI float bf2f(unsigned short v) { return __uint_as_float(((unsigned)v) << 16); }
DI float bflo(unsigned v) { return __uint_as_float(v << 16); }
DI float bfhi(unsigned v) { return __uint_as_float(v & 0xffff0000u); }
DI unsigned pk2(float a, float b) {
  f32x2 t = {a, b};
  return __builtin_bit_cast(unsigned, __builtin_convertvector(t, bf16x2));
}
DI float rbf(float a) { return bflo(pk2(a, a) & 0xffffu); }
DI s16x4 trread(const char LAS* p) {
  return __builtin_bit_cast(s16x4, __builtin_amdgcn_ds_read_tr16_b64_v4i16((LAS s16x4*)p));
}
DI bf16x8 cat8(s16x4 a, s16x4 b) {
  return __builtin_bit_cast(bf16x8, __builtin_shufflevector(a, b, 0, 1, 2, 3, 4, 5, 6, 7));
}
#define MFMA16(a, b, c) __builtin_amdgcn_mfma_f32_16x16x32_bf16((a), (b), (c), 0, 0, 0)
#define MFMA32(a, b, c) __builtin_amdgcn_mfma_f32_32x32x16_bf16((a), (b), (c), 0, 0, 0)
#define MFMA32K8(a, b, c) __builtin_amdgcn_mfma_f32_32x32x8bf16_1k((a), (b), (c), 0, 0, 0)
DI void glds16(const void* g, char LAS* l) {
  __builtin_amdgcn_global_load_lds((const unsigned*)g, (LAS unsigned*)l, 16, 0, 0);
}
DI float silu_f(float v) { return v / (1.0f + __expf(-v)); }

constexpr int KA_XSTRIDE = 1344;
constexpr int KA_W = 0;
constexpr int KA_X = 51264;
constexpr int KA_RED = KA_X + 8 * KA_XSTRIDE;
constexpr int KA_LDS = KA_RED + 128;

__global__ __launch_bounds__(256) void ka(const u16* __restrict__ x, const u16* __restrict__ wk,
                                          const u16* __restrict__ wv, const u16* __restrict__ wr,
                                          char* __restrict__ ws) {
  __shared__ __attribute__((aligned(16))) char smem_[KA_LDS];
  char LAS* smem = (char LAS*)smem_;
  const int tid = threadIdx.x, lane = tid & 63;
  const int w = __builtin_amdgcn_readfirstlane(tid >> 6);
  const int half = lane >> 5, l32 = lane & 31;
  const int tl = 2 * w + half;
  const int token = blockIdx.x * 8 + tl;

  u32x2 xv[5], wv5[5];
#pragma unroll
  for (int i = 0; i < 5; ++i) xv[i] = *(const u32x2*)(x + (size_t)token * D_ + i * 128 + l32 * 4);
#pragma unroll
  for (int i = 0; i < 5; ++i) wv5[i] = *(const u32x2*)(wr + i * 128 + l32 * 4);
  for (int j = w; j < 50; j += 4) {
    const char* src = (j < 25) ? ((const char*)wk + j * 1024) : ((const char*)wv + (j - 25) * 1024);
    glds16(src + lane * 16, smem + KA_W + j * 1024);
  }
  float f[20];
#pragma unroll
  for (int i = 0; i < 5; ++i) {
    f[4 * i + 0] = bflo(xv[i][0]); f[4 * i + 1] = bfhi(xv[i][0]);
    f[4 * i + 2] = bflo(xv[i][1]); f[4 * i + 3] = bfhi(xv[i][1]);
  }
  float s = 0.f;
#pragma unroll
  for (int i = 0; i < 20; ++i) s += f[i];
#pragma unroll
  for (int o = 1; o < 32; o <<= 1) s += __shfl_xor(s, o);
  const float mean = s * (1.0f / D_);
  float v = 0.f;
#pragma unroll
  for (int i = 0; i < 20; ++i) { float d = f[i] - mean; v += d * d; }
#pragma unroll
  for (int o = 1; o < 32; o <<= 1) v += __shfl_xor(v, o);
  const float mb = rbf(mean);
  const float vb = rbf(v * (1.0f / D_));
  const float sd = rbf(sqrtf(rbf(vb + 1e-5f)));
  float racc = 0.f;
  u16* xn = (u16*)(ws + WS_XN);
#pragma unroll
  for (int i = 0; i < 5; ++i) {
    float a0 = rbf(f[4 * i + 0] - mb) / sd, a1 = rbf(f[4 * i + 1] - mb) / sd;
    float a2 = rbf(f[4 * i + 2] - mb) / sd, a3 = rbf(f[4 * i + 3] - mb) / sd;
    u32x2 p = {pk2(a0, a1), pk2(a2, a3)};
    *(u32x2*)(xn + (size_t)token * D_ + i * 128 + l32 * 4) = p;
    *(LAS u32x2*)(smem + KA_X + tl * KA_XSTRIDE + i * 256 + l32 * 8) = p;
    racc += bflo(p[0]) * bflo(wv5[i][0]) + bfhi(p[0]) * bfhi(wv5[i][0]) + bflo(p[1]) * bflo(wv5[i][1]) +
            bfhi(p[1]) * bfhi(wv5[i][1]);
  }
#pragma unroll
  for (int o = 1; o < 32; o <<= 1) racc += __shfl_xor(racc, o);
  LAS float* red = (LAS float*)(smem + KA_RED);
  if (l32 == 0) red[tl] = racc;
  asm volatile("s_waitcnt vmcnt(0)" ::: "memory");
  __syncthreads();

  const int g = lane >> 4, c = lane & 15, q = c >> 2, pp = lane & 3;
  const int mat = w >> 1, nt = w & 1;
  const char LAS* wb = smem + KA_W + mat * 25600 + (8 * g + q) * 40 + nt * 32 + 8 * pp;
  const char LAS* xb = smem + KA_X + (c & 7) * KA_XSTRIDE + g * 16;
  f32x4 acc0 = {0, 0, 0, 0}, acc1 = {0, 0, 0, 0};
#pragma unroll
  for (int s2 = 0; s2 < 20; ++s2) {
    bf16x8 wf = cat8(trread(wb + s2 * 1280), trread(wb + s2 * 1280 + 160));
    bf16x8 xf = *(const LAS bf16x8*)(xb + s2 * 64);
    if (mat == 0) {
      if (s2 & 1) acc1 = MFMA16(wf, xf, acc1); else acc0 = MFMA16(wf, xf, acc0);
    } else {
      if (s2 & 1) acc1 = MFMA16(xf, wf, acc1); else acc0 = MFMA16(xf, wf, acc0);
    }
  }
  acc0 += acc1;
  u32x2 pk = {pk2(acc0[0], acc0[1]), pk2(acc0[2], acc0[3])};
  const int tok0 = blockIdx.x * 8;
  const int tile = tok0 >> 5;
  if (mat == 0) {
    float n2 = bflo(pk[0]) * bflo(pk[0]) + bfhi(pk[0]) * bfhi(pk[0]) + bflo(pk[1]) * bflo(pk[1]) +
               bfhi(pk[1]) * bfhi(pk[1]);
    const int r = (tok0 & 31) + c;
    if (nt == 0) {
      if (c < 8) *(u32x2*)(ws + WS_KF1 + tile * 1024 + (r + 32 * (g >> 1)) * 16 + (g & 1) * 8) = pk;
      n2 += __shfl_xor(n2, 16);
      n2 += __shfl_xor(n2, 32);
      if (lane < 8) red[8 + lane] = n2;
    } else {
      if (lane < 8) { *(u32x2*)(ws + WS_KF2 + tile * 256 + r * 8) = pk; red[16 + lane] = n2; }
    }
  } else {
    const int d = 16 * nt + c;
    const int u = (tok0 & 31) >> 3;
    if (g < 2 && d < 20)
      *(u32x2*)(ws + WS_VF + (size_t)((tile * 2 + (u >> 1)) * 2 + g) * 320 + d * 16 + (u & 1) * 8) = pk;
  }
  __syncthreads();
  if (tid == 0) {
    float rm = red[0]; int ri = 0; float km = red[8] + red[16];
    for (int i = 1; i < 8; ++i) {
      float rr = red[i];
      if (rr > rm) { rm = rr; ri = i; }
      km = fmaxf(km, red[8 + i] + red[16 + i]);
    }
    f32x4 o = {rm, __int_as_float(tok0 + ri), km, 0.f};
    *(f32x4*)(ws + WS_SUM + blockIdx.x * 16) = o;
  }
}

DI void stage_w(const u16* __restrict__ W, int hg, char LAS* dst, int w, int lane) {
#pragma unroll
  for (int i = 0; i < 13; ++i) {
    const int j = w + 8 * i;
    if (j < 100) {
      const int n = j * 64 + lane;
      const int p = n / 10, ch = n - p * 10;
      const int k = (p & ~12) | ((p & 4) << 1) | ((p & 8) >> 1);
      glds16((const char*)W + (size_t)k * 1280 + hg * 160 + ch * 16, dst + j * 1024);
    }
  }
}
template <int NT>
DI void gemm_tile(const bf16x8 (&af)[20], const char LAS* wimg, int nt0, int lane, f32x4 (&acc)[3]) {
  const int g = lane >> 4, q = (lane & 15) >> 2, pp = lane & 3;
  const char LAS* wb = wimg + (16 * (g >> 1) + 4 * (g & 1) + q) * 160 + 8 * pp + nt0 * 32;
#pragma unroll
  for (int s = 0; s < 20; ++s) {
#pragma unroll
    for (int i = 0; i < NT; ++i) {
      bf16x8 wf = cat8(trread(wb + s * 5120 + i * 32), trread(wb + s * 5120 + 1280 + i * 32));
      acc[i] = MFMA16(wf, af[s], acc[i]);
    }
  }
}
DI void read_summary(const char* __restrict__ ws, int lane, float& rmax, int& ridx, float& kn2) {
  rmax = -INFINITY; ridx = 0x7fffffff; kn2 = 0.f;
#pragma unroll
  for (int i = 0; i < 4; ++i) {
    f32x4 v = *(const f32x4*)(ws + WS_SUM + (size_t)(i * 64 + lane) * 16);
    int id = __float_as_int(v[1]);
    if (v[0] > rmax || (v[0] == rmax && id < ridx) || ridx == 0x7fffffff) { rmax = v[0]; ridx = id; }
    kn2 = fmaxf(kn2, v[2]);
  }
#pragma unroll
  for (int o = 1; o < 64; o <<= 1) {
    float r2 = __shfl_xor(rmax, o); int i2 = __shfl_xor(ridx, o); float k2 = __shfl_xor(kn2, o);
    if (r2 > rmax || (r2 == rmax && i2 < ridx)) { rmax = r2; ridx = i2; }
    kn2 = fmaxf(kn2, k2);
  }
  ridx = __builtin_amdgcn_readfirstlane(ridx);
  ridx = ridx < 0 ? 0 : (ridx >= S_ ? S_ - 1 : ridx);
}

constexpr int KB_W = 0;
constexpr int KB_RING = 0;
constexpr int KB_MAXS = 24576;
constexpr int KB_GU = 102400;
constexpr int KB_Q = 122880;
constexpr int KB_MISC = KB_Q + 12288;
constexpr int KB_LDS = KB_MISC + 64;
constexpr float QSCALE = 0.22360679774997896f * 1.4426950408889634f;

struct KVRegs { bf16x8 k1[8]; s16x4 k2[8]; bf16x8 v[8][2]; };

DI void load_kv(const char* __restrict__ ws, int w, int lane, KVRegs& kv) {
  const int r = lane & 31, h = lane >> 5;
#pragma unroll
  for (int t = 0; t < 8; ++t) {
    const int tile = w * 8 + t;
    kv.k1[t] = *(const bf16x8*)(ws + WS_KF1 + tile * 1024 + lane * 16);
    s16x4 k2 = {0x3f80, 0x3f80, 0, 0};
    if (h == 0) k2 = *(const s16x4*)(ws + WS_KF2 + tile * 256 + r * 8);
    kv.k2[t] = k2;
#pragma unroll
    for (int s = 0; s < 2; ++s) {
      const short one = (r == 20) ? (short)0x3f80 : (short)0;
      s16x8 vv = {one, one, one, one, one, one, one, one};
      if (r < 20) vv = *(const s16x8*)(ws + WS_VF + (size_t)((tile * 2 + s) * 2 + h) * 320 + r * 16);
      kv.v[t][s] = __builtin_bit_cast(bf16x8, vv);
    }
  }
}
DI bf16x8 exp_pack(const f32x16& s, int b) {
  u32x4 p;
#pragma unroll
  for (int i = 0; i < 4; ++i)
    p[i] = pk2(__builtin_amdgcn_exp2f(s[b + 2 * i]), __builtin_amdgcn_exp2f(s[b + 2 * i + 1]));
  return __builtin_bit_cast(bf16x8, p);
}

__global__ __launch_bounds__(512, 2) void kb(const u16* __restrict__ wq, const u16* __restrict__ wg,
                                             const u16* __restrict__ wu, const u16* __restrict__ wd,
                                             char* __restrict__ ws) {
  __shared__ __attribute__((aligned(16))) char smem_[KB_LDS];
  char LAS* smem = (char LAS*)smem_;
  const int tid = threadIdx.x, lane = tid & 63;
  const int w = __builtin_amdgcn_readfirstlane(tid >> 6);
  const int bid = blockIdx.x, xcd = bid & 7, jj = bid >> 3;
  const int qb = xcd * 4 + (jj >> 3), hg = jj & 7;
  const bool has_ffn = jj < 27;
  const int fcol = (xcd * 27 + jj) * 8;
  const u16* xn = (const u16*)(ws + WS_XN);

  const int mt = w & 3, ng = w >> 2;
  bf16x8 af[20];
  {
    const char* ap = (const char*)(xn + (size_t)(qb * 64 + mt * 16 + (lane & 15)) * D_) + (lane >> 4) * 16;
#pragma unroll
    for (int s = 0; s < 20; ++s) af[s] = *(const bf16x8*)(ap + s * 64);
  }
  stage_w(wq, hg, smem + KB_W, w, lane);
  if (has_ffn) {
#pragma unroll
    for (int i = 0; i < 3; ++i) {
      const int j = w + 8 * i;
      if (j < 20) {
        const int n = j * 64 + lane, k = n >> 1;
        const u16* src = (n & 1) ? wu : wg;
        glds16(src + (size_t)k * F_ + fcol, smem + KB_GU + j * 1024);
      }
    }
  }
  float rmax, kn2; int ridx;
  read_summary(ws, lane, rmax, ridx, kn2);
  asm volatile("s_waitcnt vmcnt(0)" ::: "memory");
  __syncthreads();

  {
    f32x4 acc[3] = {{0, 0, 0, 0}, {0, 0, 0, 0}, {0, 0, 0, 0}};
    if (ng == 0) gemm_tile<3>(af, smem + KB_W, 0, lane, acc);
    else gemm_tile<2>(af, smem + KB_W, 3, lane, acc);
    const int g = lane >> 4, tok = mt * 16 + (lane & 15);
#pragma unroll
    for (int i = 0; i < 3; ++i) {
      if (ng == 0 || i < 2) {
        const int cg = 4 * (3 * ng + i) + g, hh = cg / 5, dg = cg - hh * 5;
        const int b = hh * 2 + (tok >> 5), c32 = tok & 31;
        u32x2 p = {pk2(acc[i][0] * QSCALE, acc[i][1] * QSCALE), pk2(acc[i][2] * QSCALE, acc[i][3] * QSCALE)};
        char LAS* qd = smem + KB_Q + b * 1536;
        if (dg < 4) *(LAS u32x2*)(qd + (c32 + 32 * (dg >> 1)) * 16 + (dg & 1) * 8) = p;
        else *(LAS u32x2*)(qd + 1024 + c32 * 8) = p;
      }
    }
  }
  if (has_ffn && w == 7) {
    const int g = lane >> 4, q = (lane & 15) >> 2, pp = lane & 3;
    const char* xp = (const char*)(xn + (size_t)ridx * D_) + g * 16;
    const char LAS* gb = smem + KB_GU + (8 * g + q) * 32 + 8 * pp;
    f32x4 a0 = {0, 0, 0, 0}, a1 = {0, 0, 0, 0};
#pragma unroll
    for (int s = 0; s < 20; ++s) {
      bf16x8 xf = *(const bf16x8*)(xp + s * 64);
      bf16x8 wf = cat8(trread(gb + s * 1024), trread(gb + s * 1024 + 128));
      if (s & 1) a1 = MFMA16(xf, wf, a1); else a0 = MFMA16(xf, wf, a0);
    }
    const float gu = rbf(a0[0] + a1[0]);
    const float up = __shfl(gu, (lane & 7) + 8);
    const float hval = rbf(rbf(silu_f(gu)) * up);
    float* yp = (float*)(ws + WS_YP) + (size_t)(xcd * 27 + jj) * D_;
    float hc[8];
#pragma unroll
    for (int c = 0; c < 8; ++c) hc[c] = __shfl(hval, c);
#pragma unroll
    for (int rep = 0; rep < 2; ++rep) {
      const int ch = rep * 64 + lane;
      if (ch < 80) {
        float y[8] = {0, 0, 0, 0, 0, 0, 0, 0};
#pragma unroll
        for (int c = 0; c < 8; ++c) {
          u32x4 wv = *(const u32x4*)(wd + (size_t)(fcol + c) * D_ + ch * 8);
#pragma unroll
          for (int e = 0; e < 4; ++e) { y[2 * e] += hc[c] * bflo(wv[e]); y[2 * e + 1] += hc[c] * bfhi(wv[e]); }
        }
        *(f32x4*)(yp + ch * 8) = f32x4{y[0], y[1], y[2], y[3]};
        *(f32x4*)(yp + ch * 8 + 4) = f32x4{y[4], y[5], y[6], y[7]};
      }
    }
  }
  KVRegs kv;
  load_kv(ws, w, lane, kv);
  __syncthreads();
  if (tid < 256) {
    char LAS* qd = smem + KB_Q + (tid >> 5) * 1536;
    const int c32 = tid & 31;
    u32x4 a = *(LAS u32x4*)(qd + c32 * 16), b = *(LAS u32x4*)(qd + (c32 + 32) * 16);
    u32x2 c = *(LAS u32x2*)(qd + 1024 + c32 * 8);
    float n2 = 0.f;
#pragma unroll
    for (int e = 0; e < 4; ++e)
      n2 += bflo(a[e]) * bflo(a[e]) + bfhi(a[e]) * bfhi(a[e]) + bflo(b[e]) * bflo(b[e]) + bfhi(b[e]) * bfhi(b[e]);
#pragma unroll
    for (int e = 0; e < 2; ++e) n2 += bflo(c[e]) * bflo(c[e]) + bfhi(c[e]) * bfhi(c[e]);
    const float m = sqrtf(n2 * kn2) * 1.01f;
    u32x2 bias = {pk2(-m, 0.f), 0u};
    *(LAS u32x2*)(qd + 1024 + (c32 + 32) * 8) = bias;
  }
  if (tid == 0) *(LAS int*)(smem + KB_MISC) = 0;
  __syncthreads();

  const int c32 = lane & 31, h = lane >> 5;
  const f32x16 zero16 = {0, 0, 0, 0, 0, 0, 0, 0, 0, 0, 0, 0, 0, 0, 0, 0};
  f32x16 o;
  for (int attempt = 0;; ++attempt) {
    for (int st = 0; st < 8; ++st) {
      const int b = (w + st) & 7;
      const bf16x8 q1 = *(const LAS bf16x8*)(smem + KB_Q + b * 1536 + lane * 16);
      const s16x4 q2 = *(const LAS s16x4*)(smem + KB_Q + b * 1536 + 1024 + lane * 8);
      o = zero16;
#pragma unroll
      for (int t = 0; t < 8; ++t) {
        f32x16 s = MFMA32(kv.k1[t], q1, zero16);
        s = MFMA32K8(kv.k2[t], q2, s);
        bf16x8 p0 = exp_pack(s, 0), p1 = exp_pack(s, 8);
        o = MFMA32(kv.v[t][0], p0, o);
        o = MFMA32(kv.v[t][1], p1, o);
        if (t == 3) __syncthreads();
      }
      LAS f32x4* slot = (LAS f32x4*)(smem + KB_RING + b * 3072) + lane;
      if (st > 0) {
#pragma unroll
        for (int gq = 0; gq < 3; ++gq) {
          f32x4 in = slot[gq * 64];
#pragma unroll
          for (int e = 0; e < 4; ++e) o[4 * gq + e] += in[e];
        }
      }
      if (st < 7) {
#pragma unroll
        for (int gq = 0; gq < 3; ++gq) slot[gq * 64] = f32x4{o[4 * gq], o[4 * gq + 1], o[4 * gq + 2], o[4 * gq + 3]};
      }
    }
    const float l = __shfl(o[8], c32 + 32);
    if (attempt == 1) break;
    const bool bad = !(l >= 5.4e-20f && l <= 1e30f);
    if (__any(bad) && lane == 0) *(LAS int*)(smem + KB_MISC) = 1;
    __syncthreads();
    if (*(volatile LAS int*)(smem + KB_MISC) == 0) break;
    for (int b = 0; b < 8; ++b) {
      const bf16x8 q1 = *(const LAS bf16x8*)(smem + KB_Q + b * 1536 + lane * 16);
      s16x4 q2 = *(const LAS s16x4*)(smem + KB_Q + b * 1536 + 1024 + lane * 8);
      if (h == 1) q2 = s16x4{0, 0, 0, 0};
      float mx = -INFINITY;
#pragma unroll
      for (int t = 0; t < 8; ++t) {
        f32x16 s = MFMA32(kv.k1[t], q1, zero16);
        s = MFMA32K8(kv.k2[t], q2, s);
#pragma unroll
        for (int e = 0; e < 16; ++e) mx = fmaxf(mx, s[e]);
      }
      mx = fmaxf(mx, __shfl_xor(mx, 32));
      if (h == 0) *(LAS float*)(smem + KB_MAXS + (w * 256 + b * 32 + c32) * 4) = mx;
    }
    __syncthreads();
    if (tid < 256) {
      float mx = -INFINITY;
#pragma unroll
      for (int i = 0; i < 8; ++i) mx = fmaxf(mx, *(LAS float*)(smem + KB_MAXS + (i * 256 + tid) * 4));
      const float hi = rbf(mx), lo = mx - hi;
      u32x2 bias = {pk2(-hi, -lo), 0u};
      *(LAS u32x2*)(smem + KB_Q + (tid >> 5) * 1536 + 1024 + ((tid & 31) + 32) * 8) = bias;
    }
    __syncthreads();
  }
  {
    const int b = (w + 7) & 7, hh = b >> 1, tok = qb * 64 + (b & 1) * 32 + c32;
    const float l = __shfl(o[8], c32 + 32);
    const float inv = 1.0f / l;
    u16* op = (u16*)(ws + WS_O) + (size_t)tok * D_ + hg * 80 + hh * 20 + 4 * h;
    *(u32x2*)(op) = u32x2{pk2(o[0] * inv, o[1] * inv), pk2(o[2] * inv, o[3] * inv)};
    *(u32x2*)(op + 8) = u32x2{pk2(o[4] * inv, o[5] * inv), pk2(o[6] * inv, o[7] * inv)};
    if (h == 0) *(u32x2*)(op + 16) = u32x2{pk2(o[8] * inv, o[9] * inv), pk2(o[10] * inv, o[11] * inv)};
  }
}

constexpr int KC_W = 0;
constexpr int KC_Y = 102400;
constexpr int KC_LDS = KC_Y + 2560;

__global__ __launch_bounds__(512, 2) void kc(const u16* __restrict__ x, const u16* __restrict__ wo,
                                             const char* __restrict__ ws, u16* __restrict__ out) {
  __shared__ __attribute__((aligned(16))) char smem_[KC_LDS];
  char LAS* smem = (char LAS*)smem_;
  const int tid = threadIdx.x, lane = tid & 63;
  const int w = __builtin_amdgcn_readfirstlane(tid >> 6);
  const int bid = blockIdx.x, xcd = bid & 7, jj = bid >> 3;
  const int qb = xcd * 4 + (jj >> 3), hg = jj & 7;
  const u16* xn = (const u16*)(ws + WS_XN);
  const u16* ob = (const u16*)(ws + WS_O);
  const int mt = w & 3, ng = w >> 2;
  const int g = lane >> 4, tok = qb * 64 + mt * 16 + (lane & 15);
  bf16x8 af[20];
  {
    const char* ap = (const char*)(ob + (size_t)tok * D_) + g * 16;
#pragma unroll
    for (int s = 0; s < 20; ++s) af[s] = *(const bf16x8*)(ap + s * 64);
  }
  stage_w(wo, hg, smem + KC_W, w, lane);
  u32x2 xe[3], xne[3];
#pragma unroll
  for (int i = 0; i < 3; ++i) {
    if (ng == 0 || i < 2) {
      const size_t off = (size_t)tok * D_ + hg * 80 + (3 * ng + i) * 16 + 4 * g;
      xe[i] = *(const u32x2*)(x + off);
      xne[i] = *(const u32x2*)(xn + off);
    }
  }
  float rmax, kn2; int ridx;
  read_summary(ws, lane, rmax, ridx, kn2);
  const bool sel_blk = (ridx >> 6) == qb;
  LAS float* ys = (LAS float*)(smem + KC_Y);
  if (sel_blk) {
    if (tid < 480) {
      const int col = tid % 80, part = tid / 80;
      const float* yp = (const float*)(ws + WS_YP) + hg * 80 + col;
      float a = 0.f;
#pragma unroll 12
      for (int i = 0; i < 36; ++i) a += yp[(size_t)(part * 36 + i) * D_];
      ys[part * 80 + col] = a;
    }
    __syncthreads();
    float yv = 0.f;
    if (tid < 80) {
      float a = 0.f;
#pragma unroll
      for (int i = 0; i < 6; ++i) a += ys[i * 80 + tid];
      const float rs = rbf(1.0f / (1.0f + __expf(-rbf(rmax))));
      yv = rbf(rbf(a) * rs);
    }
    __syncthreads();
    if (tid < 80) ys[tid] = yv;
  }
  asm volatile("s_waitcnt vmcnt(0)" ::: "memory");
  __syncthreads();
  f32x4 acc[3] = {{0, 0, 0, 0}, {0, 0, 0, 0}, {0, 0, 0, 0}};
  if (ng == 0) gemm_tile<3>(af, smem + KC_W, 0, lane, acc);
  else gemm_tile<2>(af, smem + KC_W, 3, lane, acc);
#pragma unroll
  for (int i = 0; i < 3; ++i) {
    if (ng == 0 || i < 2) {
      const int n0 = (3 * ng + i) * 16 + 4 * g;
      float xv[4] = {bflo(xe[i][0]), bfhi(xe[i][0]), bflo(xe[i][1]), bfhi(xe[i][1])};
      float bv[4] = {bflo(xne[i][0]), bfhi(xne[i][0]), bflo(xne[i][1]), bfhi(xne[i][1])};
      if (tok == ridx) {
#pragma unroll
        for (int e = 0; e < 4; ++e) bv[e] = rbf(bv[e] + ys[n0 + e]);
      }
      float r[4];
#pragma unroll
      for (int e = 0; e < 4; ++e) r[e] = xv[e] + rbf(rbf(acc[i][e]) * rbf(silu_f(bv[e])));
      *(u32x2*)(out + (size_t)tok * D_ + hg * 80 + n0) = u32x2{pk2(r[0], r[1]), pk2(r[2], r[3])};
    }
  }
}

extern "C" void kernel_launch(void* const* d_in, const int* in_sizes, int n_in, void* d_out, int out_size,
                              void* d_ws, size_t ws_size, hipStream_t stream) {
  const u16* x = (const u16*)d_in[0];
  const u16* wq = (const u16*)d_in[1];
  const u16* wk = (const u16*)d_in[2];
  const u16* wv = (const u16*)d_in[3];
  const u16* wo = (const u16*)d_in[4];
  const u16* wr = (const u16*)d_in[6];
  const u16* wg = (const u16*)d_in[7];
  const u16* wu = (const u16*)d_in[8];
  const u16* wd = (const u16*)d_in[9];
  char* ws = (char*)d_ws;
  ka<<<256, 256, 0, stream>>>(x, wk, wv, wr, ws);
  kb<<<256, 512, 0, stream>>>(wq, wg, wu, wd, ws);
  kc<<<256, 512, 0, stream>>>(x, wo, ws, (u16*)d_out);
}
```

```cpp
#include <hip/hip_runtime.h>
#include <hip/hip_bf16.h>
#include <stdint.h>

typedef __attribute__((ext_vector_type(8))) __bf16 bf16x8;
typedef __attribute__((ext_vector_type(2))) __bf16 bf16x2;
typedef __attribute__((ext_vector_type(4))) short s16x4;
typedef __attribute__((ext_vector_type(2))) float f32x2;
typedef __attribute__((ext_vector_type(4))) float f32x4;
typedef __attribute__((ext_vector_type(16))) float f32x16;
typedef __attribute__((ext_vector_type(2))) unsigned u32x2;
typedef __attribute__((ext_vector_type(4))) unsigned u32x4;
typedef unsigned short u16;
#define LAS __attribute__((address_space(3)))
#define DI __device__ __forceinline__
#define SB() __builtin_amdgcn_sched_barrier(0)
#define CFENCE() asm volatile("" ::: "memory")

constexpr int S_ = 2048, D_ = 640, F_ = 1728;

constexpr int WS_O = 0;
constexpr int WS_KF1 = WS_O + S_ * D_ * 2;
constexpr int WS_KF2 = WS_KF1 + 65536;
constexpr int WS_VF = WS_KF2 + 64 * 272;
constexpr int WS_YP = WS_VF + 64 * 1408;
constexpr int WS_FC = WS_YP + 216 * D_ * 4;
constexpr int WS_FB = WS_FC + 4096;
constexpr int WS_FY = WS_FB + 4096;
constexpr int WS_EP = WS_FY + 1024;
constexpr int WS_BYTES = WS_EP + 256;

constexpr int L_W = 0;
constexpr int L_SCR = 101376;
constexpr int L_WK = 102400, L_WV = 128000, L_X = 153600;
constexpr int L_WHI = 112640;
constexpr int L_RING = 51200;
constexpr int L_Q = 100352;
constexpr int L_PART = 112640;
constexpr int L_G = 112640, L_U = 122880, L_DN = 133120;
constexpr int L_FRED = 143360;
constexpr int L_XS = 161280;
constexpr int L_SUM = 162560;
constexpr int L_STAT = 155648;
constexpr int L_CS = 156672;
constexpr int L_STP = 159232;
constexpr int L_MAXS = L_RING;
constexpr int L_PART2 = L_RING;
constexpr int L_XSUM = L_RING + 43008;
constexpr int L_YS = L_RING + 48768;
constexpr int L_CNT = L_Q + 1024 + 256 + 4;
constexpr int LDS_BYTES = 163840;

constexpr float QSCALE = 0.22360679774997896f * 1.4426950408889634f;
constexpr unsigned SPIN_CAP = 1u << 18;

DI float bflo(unsigned v) { return __uint_as_float(v << 16); }
DI float bfhi(unsigned v) { return __uint_as_float(v & 0xffff0000u); }
DI unsigned pk2(float a, float b) {
  f32x2 t = {a, b};
  return __builtin_bit_cast(unsigned, __builtin_convertvector(t, bf16x2));
}
DI float rbf(float a) { return bflo(pk2(a, a)); }
DI float dot2(unsigned a, unsigned b, float c) {
  return __builtin_amdgcn_fdot2_f32_bf16(__builtin_bit_cast(bf16x2, a), __builtin_bit_cast(bf16x2, b), c, false);
}
DI s16x4 trread(const char LAS* p) {
  return __builtin_bit_cast(s16x4, __builtin_amdgcn_ds_read_tr16_b64_v4i16((LAS s16x4*)p));
}
DI bf16x8 cat8(s16x4 a, s16x4 b) {
  return __builtin_bit_cast(bf16x8, __builtin_shufflevector(a, b, 0, 1, 2, 3, 4, 5, 6, 7));
}
#define MFMA16(a, b, c) __builtin_amdgcn_mfma_f32_16x16x32_bf16((a), (b), (c), 0, 0, 0)
#define MFMA32(a, b, c) __builtin_amdgcn_mfma_f32_32x32x16_bf16((a), (b), (c), 0, 0, 0)
#define MFMA32K8(a, b, c) __builtin_amdgcn_mfma_f32_32x32x8bf16_1k((a), (b), (c), 0, 0, 0)
#define EXP2(x) __builtin_amdgcn_exp2f(x)
DI float sigm_f(float v) { return __builtin_amdgcn_rcpf(1.0f + EXP2(v * -1.4426950408889634f)); }
DI float silu_f(float v) { return v * sigm_f(v); }
DI void bar_lds() { asm volatile("s_waitcnt lgkmcnt(0)\n\ts_barrier" ::: "memory"); }
DI void bar_all() { asm volatile("s_waitcnt vmcnt(0) lgkmcnt(0)\n\ts_barrier" ::: "memory"); }
DI void drain() { asm volatile("s_waitcnt vmcnt(0)" ::: "memory"); }
DI void lds_add(LAS unsigned* p, unsigned v) {
  asm volatile("s_waitcnt lgkmcnt(0)" ::: "memory");
  __hip_atomic_fetch_add(p, v, __ATOMIC_RELAXED, __HIP_MEMORY_SCOPE_WORKGROUP);
}
DI unsigned lds_add_rtn(LAS unsigned* p, unsigned v) {
  asm volatile("s_waitcnt lgkmcnt(0)" ::: "memory");
  const unsigned r = __hip_atomic_fetch_add(p, v, __ATOMIC_RELAXED, __HIP_MEMORY_SCOPE_WORKGROUP);
  CFENCE();
  return r;
}
DI unsigned lds_get(LAS unsigned* p) {
  CFENCE();
  const unsigned r = __builtin_amdgcn_readfirstlane(__hip_atomic_load(p, __ATOMIC_RELAXED, __HIP_MEMORY_SCOPE_WORKGROUP));
  CFENCE();
  return r;
}
DI void lds_wait(LAS unsigned* p, unsigned target) {
#pragma unroll 1
  while (lds_get(p) < target) __builtin_amdgcn_s_sleep(1);
}
DI void dma16(const void* sbase, unsigned voff, unsigned lds) {
  unsigned keep;
  asm volatile("s_mov_b32 %0, m0\n\ts_mov_b32 m0, %3\n\ts_nop 0\n\tglobal_load_lds_dwordx4 %1, %2\n\ts_mov_b32 m0, %0"
               : "=&s"(keep) : "v"(voff), "s"(sbase), "s"(lds) : "memory");
}
DI void dma16_if(unsigned on, const void* sbase, unsigned voff, unsigned lds) {
  unsigned keep;
  on = __builtin_amdgcn_readfirstlane(on);
  lds = __builtin_amdgcn_readfirstlane(lds);
  sbase = (const void*)(((unsigned long long)__builtin_amdgcn_readfirstlane((unsigned)((unsigned long long)sbase >> 32)) << 32) |
                        (unsigned)__builtin_amdgcn_readfirstlane((unsigned)(unsigned long long)sbase));
  asm volatile("s_cmp_eq_u32 %4, 0\n\ts_cbranch_scc1 1f\n\ts_mov_b32 %0, m0\n\ts_mov_b32 m0, %3\n\ts_nop 1\n\t"
               "global_load_lds_dwordx4 %1, %2\n\ts_mov_b32 m0, %0\n1:"
               : "=&s"(keep) : "v"(voff), "s"(sbase), "s"(lds), "s"(on) : "memory", "scc");
}
typedef __amdgpu_buffer_rsrc_t rsrc_t;
DI u32x4 ld16(rsrc_t r, int v, int s = 0) { return __builtin_amdgcn_raw_buffer_load_b128(r, v, s, 16); }
DI u32x2 ld8(rsrc_t r, int v, int s = 0) { return __builtin_amdgcn_raw_buffer_load_b64(r, v, s, 16); }
DI unsigned ld4(rsrc_t r, int v, int s = 0) { return __builtin_amdgcn_raw_buffer_load_b32(r, v, s, 16); }
DI void st16(rsrc_t r, u32x4 d, int v, int s = 0) { __builtin_amdgcn_raw_buffer_store_b128(d, r, v, s, 16); }
DI void st8(rsrc_t r, u32x2 d, int v, int s = 0) { __builtin_amdgcn_raw_buffer_store_b64(d, r, v, s, 16); }
DI void st4(rsrc_t r, unsigned d, int v, int s = 0) { __builtin_amdgcn_raw_buffer_store_b32(d, r, v, s, 16); }

template <int CTRL> DI float dpp_mov(float v) {
  return __builtin_bit_cast(float, __builtin_amdgcn_update_dpp(0, __builtin_bit_cast(int, v), CTRL, 0xf, 0xf, true));
}
DI float sum16(float v) {
  v += dpp_mov<0xB1>(v); v += dpp_mov<0x4E>(v); v += dpp_mov<0x141>(v); v += dpp_mov<0x140>(v);
  return v;
}
DI float sum_rows(float v) {
  const unsigned b = __float_as_uint(v);
  auto r = __builtin_amdgcn_permlane16_swap(b, b, false, false);
  return __uint_as_float(r[0]) + __uint_as_float(r[1]);
}
DI float sum_halves(float v) {
  const unsigned b = __float_as_uint(v);
  auto r = __builtin_amdgcn_permlane32_swap(b, b, false, false);
  return __uint_as_float(r[0]) + __uint_as_float(r[1]);
}
DI float hi_half(float v) {
  const unsigned b = __float_as_uint(v);
  auto r = __builtin_amdgcn_permlane32_swap(b, b, false, false);
  return __uint_as_float(r[1]);
}
DI float max64(float v) {
  v = fmaxf(v, dpp_mov<0xB1>(v)); v = fmaxf(v, dpp_mov<0x4E>(v)); v = fmaxf(v, dpp_mov<0x141>(v)); v = fmaxf(v, dpp_mov<0x140>(v));
  auto r = __builtin_amdgcn_permlane16_swap(__float_as_uint(v), __float_as_uint(v), false, false);
  v = fmaxf(__uint_as_float(r[0]), __uint_as_float(r[1]));
  auto q = __builtin_amdgcn_permlane32_swap(__float_as_uint(v), __float_as_uint(v), false, false);
  return fmaxf(__uint_as_float(q[0]), __uint_as_float(q[1]));
}
DI int opaque(int v) { asm volatile("" : "+v"(v)); return v; }
DI float sum32(float v) { return sum_rows(sum16(v)); }
DI float rdlane(float v, int l) { return __builtin_bit_cast(float, __builtin_amdgcn_readlane(__builtin_bit_cast(int, v), l)); }

DI float ln_sd(float var) { return rbf(sqrtf(rbf(rbf(var) + 1e-5f))); }
DI unsigned ln_pair(unsigned xp, float mb, float sd, float rc) {
  const unsigned dp = pk2(bflo(xp) - mb, bfhi(xp) - mb);
  const f32x2 d = {bflo(dp), bfhi(dp)};
  const f32x2 q0 = d * rc;
  const f32x2 a = __builtin_elementwise_fma(__builtin_elementwise_fma(-q0, f32x2{sd, sd}, d), f32x2{rc, rc}, q0);
  return pk2(a[0], a[1]);
}
DI void load_piece(const u16* __restrict__ xrow0, int lane, u32x4 (&xp)[10]) {
#pragma unroll
  for (int i = 0; i < 10; ++i) {
    const int n = i * 64 + lane, row = n / 40, ch = n - row * 40;
    xp[i] = *(const u32x4*)((const char*)xrow0 + row * 1280 + ch * 16);
  }
}
DI void bounce_piece(char LAS* blk, int lane, const u32x4 (&xp)[10], bf16x8 (&af)[10]) {
#pragma unroll
  for (int i = 0; i < 10; ++i) {
    const int n = i * 64 + lane, row = n / 40, ch = n - row * 40;
    *(LAS u32x4*)(blk + row * 656 + ch * 16) = xp[i];
  }
#pragma unroll
  for (int s = 0; s < 10; ++s) af[s] = *(const LAS bf16x8*)(blk + (lane & 15) * 656 + (lane >> 4) * 16 + s * 64);
}
DI void w_voff(int lane, unsigned (&voff)[5]) {
#pragma unroll
  for (int i = 0; i < 5; ++i) {
    const int n = i * 64 + lane, p = n / 10, ch = n - p * 10;
    const int k = (p & ~12) | ((p & 4) << 1) | ((p & 8) >> 1);
    voff[i] = k * 1280 + ch * 16;
  }
}
template <class F>
DI void gemm_half(bf16x8 (&af)[10], const char LAS* wimg, int lane, f32x4 (&acc)[5], F side) {
  const int g = lane >> 4, q = (lane & 15) >> 2, pp = lane & 3;
  const char LAS* wb = wimg + (16 * (g >> 1) + 4 * (g & 1) + q) * 160 + 8 * pp;
  bf16x8 wf[3][5];
#pragma unroll
  for (int p = 0; p < 2; ++p)
#pragma unroll
    for (int i = 0; i < 5; ++i) wf[p][i] = cat8(trread(wb + p * 5120 + i * 32), trread(wb + p * 5120 + 1280 + i * 32));
#pragma unroll
  for (int s = 0; s < 10; ++s) {
#pragma unroll
    for (int i = 0; i < 5; ++i) {
      acc[i] = MFMA16(wf[s % 3][i], af[s], acc[i]);
      SB();
      if (s < 8) wf[(s + 2) % 3][i] = cat8(trread(wb + (s + 2) * 5120 + i * 32), trread(wb + (s + 2) * 5120 + 1280 + i * 32));
      SB();
    }
    side(s, wf[s % 3]);
    SB();
  }
}
DI void write_part(char LAS* part, int kh, int mt, int lane, const f32x4 (&acc)[5]) {
  char LAS* p = part + kh * 21504 + (mt * 16 + (lane & 15)) * 336 + (lane >> 4) * 16;
#pragma unroll
  for (int i = 0; i < 5; ++i) *(LAS f32x4*)(p + i * 64) = acc[i];
}

struct KVRegs { bf16x8 k1[8]; s16x4 k2[8]; bf16x8 v[8][2]; };
struct DmaJob { const char* sb; unsigned sstr, lds, on; unsigned voff[5]; };

#define EXP_PK(dst, S, b) { dst[0] = pk2(EXP2(S[b]), EXP2(S[b + 1])); dst[1] = pk2(EXP2(S[b + 2]), EXP2(S[b + 3])); }
template <bool NEWBLK, int PRIO>
DI void att_step(f32x16& sc, f32x16& sn, const bf16x8& k1n, const s16x4& k2n, const bf16x8& q1n, const s16x4& q2n,
                 const bf16x8& v0, const bf16x8& v1prev, u32x4& p1, f32x16& oprev, f32x16& ocur) {
  const f32x16 zero16 = {0, 0, 0, 0, 0, 0, 0, 0, 0, 0, 0, 0, 0, 0, 0, 0};
  u32x2 a, b, c, d;
  if (PRIO >= 0) __builtin_amdgcn_s_setprio(PRIO);
  sn = MFMA32(k1n, q1n, zero16); SB();
  EXP_PK(a, sc, 0); SB();
  sn = MFMA32K8(k2n, q2n, sn); SB();
  EXP_PK(b, sc, 4); SB();
  oprev = MFMA32(v1prev, __builtin_bit_cast(bf16x8, p1), oprev); SB();
  EXP_PK(c, sc, 8); SB();
  const u32x4 p0 = {a[0], a[1], b[0], b[1]};
  if (NEWBLK) ocur = MFMA32(v0, __builtin_bit_cast(bf16x8, p0), ocur);
  else ocur = MFMA32(v0, __builtin_bit_cast(bf16x8, p0), oprev);
  SB();
  EXP_PK(d, sc, 12); SB();
  p1 = u32x4{c[0], c[1], d[0], d[1]};
}
template <class H>
DI void att_block(const KVRegs& kv, char LAS* smem, int lane, int sout, int sin, int bnext, bf16x8& q1, s16x4& q2,
                  f32x16& sa, f32x16& sb, u32x4& p1, f32x16& oprev, f32x16& ocur, H hook) {
  LAS f32x4* so = (LAS f32x4*)(smem + L_RING + sout * 3072) + lane;
  LAS f32x4* si = (LAS f32x4*)(smem + L_RING + sin * 3072) + lane;
  att_step<true, -1>(sa, sb, kv.k1[1], kv.k2[1], q1, q2, kv.v[0][0], kv.v[7][1], p1, oprev, ocur);
  hook(0); SB();
  att_step<false, 1>(sb, sa, kv.k1[2], kv.k2[2], q1, q2, kv.v[1][0], kv.v[0][1], p1, ocur, ocur);
  so[0] = f32x4{oprev[0], oprev[1], oprev[2], oprev[3]};
  hook(1); SB();
  att_step<false, -1>(sa, sb, kv.k1[3], kv.k2[3], q1, q2, kv.v[2][0], kv.v[1][1], p1, ocur, ocur);
  so[64] = f32x4{oprev[4], oprev[5], oprev[6], oprev[7]};
  hook(2); SB();
  att_step<false, 0>(sb, sa, kv.k1[4], kv.k2[4], q1, q2, kv.v[3][0], kv.v[2][1], p1, ocur, ocur);
  so[128] = f32x4{oprev[8], oprev[9], oprev[10], oprev[11]};
  hook(3); SB();
  att_step<false, -1>(sa, sb, kv.k1[5], kv.k2[5], q1, q2, kv.v[4][0], kv.v[3][1], p1, ocur, ocur);
  hook(4);
  bar_lds();
  const bf16x8 q1n = *(const LAS bf16x8*)(smem + L_Q + bnext * 1536 + lane * 16);
  const s16x4 q2n = *(const LAS s16x4*)(smem + L_Q + bnext * 1536 + 1024 + lane * 8);
  SB();
  att_step<false, 3>(sb, sa, kv.k1[6], kv.k2[6], q1, q2, kv.v[5][0], kv.v[4][1], p1, ocur, ocur);
  {
    const f32x4 i0 = si[0], i1 = si[64], i2 = si[128];
#pragma unroll
    for (int e = 0; e < 4; ++e) { oprev[e] = i0[e]; oprev[4 + e] = i1[e]; oprev[8 + e] = i2[e]; }
  }
  hook(5); SB();
  att_step<false, -1>(sa, sb, kv.k1[7], kv.k2[7], q1, q2, kv.v[6][0], kv.v[5][1], p1, ocur, ocur);
  hook(6); SB();
  att_step<false, 2>(sb, sa, kv.k1[0], kv.k2[0], q1n, q2n, kv.v[7][0], kv.v[6][1], p1, ocur, ocur);
  q1 = q1n; q2 = q2n;
}
DI void dma_hook(const DmaJob& dj, unsigned wait5, int i) {
  if (i == 4) {
    asm volatile("s_cmp_eq_u32 %0, 0\n\ts_cbranch_scc1 1f\n\ts_waitcnt vmcnt(5)\n1:" :: "s"(__builtin_amdgcn_readfirstlane(wait5)) : "memory", "scc");
  } else if (i != 1) {
    const int c = i == 0 ? 0 : (i < 4 ? i - 1 : i - 2);
    dma16_if(dj.on, dj.sb + (size_t)c * dj.sstr, dj.voff[c], dj.lds + c * 1024);
  }
}

__global__ __launch_bounds__(512, 2) void fused(const u16* __restrict__ x, const u16* __restrict__ wq,
                                                const u16* __restrict__ wk, const u16* __restrict__ wv,
                                                const u16* __restrict__ wo, const u16* __restrict__ wr,
                                                const u16* __restrict__ wg, const u16* __restrict__ wu,
                                                const u16* __restrict__ wd, char* ws, u16* __restrict__ out) {
  __shared__ __attribute__((aligned(16))) char smem_[LDS_BYTES];
  char LAS* smem = (char LAS*)smem_;
  const unsigned lds0 = (unsigned)(size_t)smem;
  const int tid = threadIdx.x, lane = tid & 63;
  const int w = __builtin_amdgcn_readfirstlane(tid >> 6);
  const int bid = blockIdx.x, xcd = bid & 7, jj = bid >> 3;
  const int qb = xcd * 4 + (jj >> 3), hg = jj & 7, pid = qb * 8 + hg;
  const bool has_ffn = jj < 27;
  const int fid = xcd * 27 + jj, fcol = fid * 8;
  const int tok0 = pid * 8, tile = tok0 >> 5;
  const rsrc_t rs = __builtin_amdgcn_make_buffer_rsrc(ws, 0, WS_BYTES, 0x00020000);
  LAS float* scr = (LAS float*)(smem + L_SCR);
  LAS unsigned* cnt = (LAS unsigned*)(smem + L_SCR + 96);


  unsigned tag = 0;
  u32x4 late = {0, 0, 0, 0};
  int late_off = -1;
  const int mt = w & 3, kh = w >> 2;
  bf16x8 af[10];
  const u16* xpiece = x + (size_t)(qb * 64 + mt * 16) * D_ + kh * 320;
  if (w < 4) {
    const int half = lane >> 5, l32 = lane & 31, tl = 2 * w + half;
    u32x2 xv[5], wv5[5];
    u32x4 xp[10];
#pragma unroll
    for (int i = 0; i < 5; ++i) xv[i] = *(const u32x2*)(x + (size_t)(tok0 + tl) * D_ + i * 128 + l32 * 4);
    const unsigned ep = ld4(rs, WS_EP);
#pragma unroll
    for (int i = 0; i < 5; ++i) wv5[i] = *(const u32x2*)(wr + i * 128 + l32 * 4);
    if (tid < 2) *((LAS u32x4*)cnt + tid) = u32x4{0, 0, 0, 0};
    bar_lds();
    u32x4 a[14];
    const int rot6 = (jj + xcd) % 6;
    const int tl16 = (tid < 64 ? tid : 63) * 16 + 24576;
#pragma unroll
    for (int i = 0; i < 6; ++i) {
      const int j = (i + rot6 >= 6 ? i + rot6 - 6 : i + rot6) * 4096;
      a[i] = *(const u32x4*)((const char*)wk + j + tid * 16);
      a[7 + i] = *(const u32x4*)((const char*)wv + j + tid * 16);
    }
    a[6] = *(const u32x4*)((const char*)wk + tl16);
    a[13] = *(const u32x4*)((const char*)wv + tl16);
    SB();
    float s = 0.f;
#pragma unroll
    for (int i = 0; i < 5; ++i) { s = dot2(xv[i][0], 0x3f803f80u, s); s = dot2(xv[i][1], 0x3f803f80u, s); }

    const float mean = sum32(s) * (1.0f / D_);
    f32x2 v2 = {0.f, 0.f};
#pragma unroll
    for (int i = 0; i < 10; ++i) {
      const f32x2 d = f32x2{bflo(xv[i >> 1][i & 1]), bfhi(xv[i >> 1][i & 1])} - mean;
      v2 += d * d;
    }
    const float sd = ln_sd(sum32(v2[0] + v2[1]) * (1.0f / D_));
    const float mb = rbf(mean), rc = 1.0f / sd;
#pragma unroll
    for (int i = 0; i < 5; ++i) {
      xv[i] = u32x2{ln_pair(xv[i][0], mb, sd, rc), ln_pair(xv[i][1], mb, sd, rc)};
      *(LAS u32x2*)(smem + L_X + tl * 1280 + (((i * 16 + (l32 >> 1)) ^ tl) * 16) + (l32 & 1) * 8) = xv[i];
    }
#pragma unroll
    for (int i = 0; i < 6; ++i) {
      const int j = (i + rot6 >= 6 ? i + rot6 - 6 : i + rot6) * 4096;
      *(LAS u32x4*)(smem + L_WK + j + tid * 16) = a[i];
      *(LAS u32x4*)(smem + L_WV + j + tid * 16) = a[7 + i];
    }
    *(LAS u32x4*)(smem + L_WK + tl16) = a[6];
    *(LAS u32x4*)(smem + L_WV + tl16) = a[13];
    if (lane == 0) lds_add(cnt + 0, 1);
    float racc = 0.f;
#pragma unroll
    for (int i = 0; i < 5; ++i) { racc = dot2(xv[i][0], wv5[i][0], racc); racc = dot2(xv[i][1], wv5[i][1], racc); }
    racc = sum32(racc);
    if (l32 == 0) scr[tl] = racc;
    tag = __builtin_amdgcn_readfirstlane(ep) + 1u;
    lds_wait(cnt + 0, 8);

    const int g = lane >> 4, c = lane & 15, q = c >> 2, pp = lane & 3, r8 = c & 7;
    const int mat = w >> 1, nt = w & 1;
    const char LAS* wb = smem + L_WK + mat * 25600 + (8 * g + q) * 40 + nt * 32 + 8 * pp;
    const char LAS* xb0 = smem + L_X + r8 * 1280 + (g ^ r8) * 16;
    const char LAS* xb1 = smem + L_X + r8 * 1280 + ((4 + g) ^ r8) * 16;
    bf16x8 wf[20], xf[20];
#pragma unroll
    for (int s2 = 0; s2 < 20; ++s2) {
      xf[s2] = *(const LAS bf16x8*)(((s2 & 1) ? xb1 : xb0) + (s2 >> 1) * 128);
      wf[s2] = cat8(trread(wb + s2 * 1280), trread(wb + s2 * 1280 + 160));
    }
    f32x4 a0 = {0, 0, 0, 0}, a1 = {0, 0, 0, 0};
    if (mat == 0) {
#pragma unroll
      for (int s2 = 0; s2 < 20; s2 += 2) { a0 = MFMA16(wf[s2], xf[s2], a0); a1 = MFMA16(wf[s2 + 1], xf[s2 + 1], a1); }
    } else {
#pragma unroll
      for (int s2 = 0; s2 < 20; s2 += 2) { a0 = MFMA16(xf[s2], wf[s2], a0); a1 = MFMA16(xf[s2 + 1], wf[s2 + 1], a1); }
    }
    if (lane == 0) lds_add(cnt + 2, 1);
    a0 += a1;
    u32x2 pk = {pk2(a0[0], a0[1]), pk2(a0[2], a0[3])};
    int soff; bool son;
    if (mat == 0) {
      float n2 = dot2(pk[0], pk[0], dot2(pk[1], pk[1], 0.f));
      const int r = (tok0 & 31) + c;
      if (nt == 0) {
        soff = WS_KF1 + tile * 1024 + (r + 32 * (g >> 1)) * 16 + (g & 1) * 8; son = c < 8;
        n2 = sum_halves(sum_rows(n2));
      } else {
        if (lane == 8) pk = u32x2{0x3f803f80u, 0u};
        soff = WS_KF2 + tile * 272 + (lane < 8 ? r : 32) * 8; son = lane <= 8;
      }
      if (lane < 8) scr[8 + nt * 8 + lane] = n2;
    } else {
      const int d = 16 * nt + c, u = (tok0 & 31) >> 3;
      if (d == 20) pk = u32x2{0x3f803f80u, 0x3f803f80u};
      if (d == 21) pk = u32x2{0u, 0u};
      soff = WS_VF + ((tile * 2 + (u >> 1)) * 2 + g) * 352 + d * 16 + (u & 1) * 8; son = g < 2 && d < 22;
    }
    if (son) st8(rs, pk, soff);
    CFENCE();
    load_piece(xpiece, lane, xp);
    asm volatile("s_waitcnt vmcnt(10)" ::: "memory");
    if (lane == 0 && lds_add_rtn(cnt + 3, 1) == 3) {
      f32x4 sv[6];
#pragma unroll
      for (int i = 0; i < 6; ++i) sv[i] = *(const LAS f32x4*)(scr + 4 * i);
      float rm = sv[0][0], km = sv[2][0] + sv[4][0]; int ri = 0;
#pragma unroll
      for (int i = 1; i < 8; ++i) {
        const float rr = sv[i >> 2][i & 3];
        const bool t = rr > rm;
        rm = t ? rr : rm; ri = t ? i : ri;
        km = fmaxf(km, sv[2 + (i >> 2)][i & 3] + sv[4 + (i >> 2)][i & 3]);
      }
      const unsigned kb = (__float_as_uint(km) + 0xffffu) & 0xffff0000u;
      st16(rs, u32x4{tag, __float_as_uint(rm), tag, kb | (unsigned)(tok0 + ri)}, WS_FB + pid * 16);
    }

    lds_wait(cnt + 2, 4);
    bounce_piece(smem + L_WK + w * 10496, lane, xp, af);
  } else {
    u32x4 xp[10];
    const unsigned ep = ld4(rs, WS_EP);
    load_piece(xpiece, lane, xp);
    unsigned voff[5];
    w_voff(lane, voff);
    if (w == 7) {
      late = *(const u32x4*)((const char*)wq + hg * 160 + 19 * 40960 + voff[4]);
      late_off = 99 * 1024 + lane * 16;
    }
    bar_lds();
    tag = __builtin_amdgcn_readfirstlane(ep) + 1u;
    asm volatile("" :: "s"(tag) : "memory");
    if (lane == 0) lds_add(cnt + 0, 1);

    bounce_piece(smem + L_W + (w - 4) * 10496, lane, xp, af);
#pragma unroll
    for (int s = 0; s < 10; ++s) asm volatile("" : "+v"(af[s]));
    asm volatile("" : "+v"(late));
    if (lane == 0) lds_add(cnt + 4, 1);
    lds_wait(cnt + 4, 4);

    int G = ((jj >> 3) * 5 + ((xcd * 5) >> 3) + (w - 4)) % 20;
#pragma unroll 1
    for (int i = 0; i < 5; ++i) {
      const char* sb = (const char*)wq + hg * 160 + (size_t)G * 40960;
#pragma unroll
      for (int c = 0; c < 5; ++c) dma16_if(G * 5 + c != 99, sb, voff[c], lds0 + L_W + G * 5120 + c * 1024);
      G = G >= 16 ? G - 16 : G + 4;
    }

  }
  float s1 = 0.f, s2q = 0.f;
#pragma unroll
  for (int s = 0; s < 10; ++s) {
    const u32x4 u = __builtin_bit_cast(u32x4, af[s]);
#pragma unroll
    for (int e = 0; e < 4; ++e) { s1 = dot2(u[e], 0x3f803f80u, s1); s2q = dot2(u[e], u[e], s2q); }
  }
  s1 = sum_halves(sum_rows(s1)); s2q = sum_halves(sum_rows(s2q));
  bar_all();


  if (late_off >= 0) *(LAS u32x4*)(smem + L_W + late_off) = late;
  if (lane < 16) *(LAS f32x2*)(smem + L_STAT + (kh * 64 + mt * 16 + lane) * 8) = f32x2{s1, s2q};
  bar_lds();

  {
    f32x4 acc[5] = {{0, 0, 0, 0}, {0, 0, 0, 0}, {0, 0, 0, 0}, {0, 0, 0, 0}, {0, 0, 0, 0}};
    f32x4 cs[5] = {{0, 0, 0, 0}, {0, 0, 0, 0}, {0, 0, 0, 0}, {0, 0, 0, 0}, {0, 0, 0, 0}};
    const bf16x8 ones = __builtin_bit_cast(bf16x8, u32x4{0x3f803f80u, 0x3f803f80u, 0x3f803f80u, 0x3f803f80u});
    gemm_half(af, smem + L_W + kh * 51200, lane, acc, [&](int t, const bf16x8 (&wf)[5]) {
      if ((t & 3) == mt) {
#pragma unroll
        for (int i = 0; i < 5; ++i) cs[i] = MFMA16(wf[i], ones, cs[i]);
      }
    });
    write_part(smem + L_PART, kh, mt, lane, acc);
    if ((lane & 15) == 0) {
#pragma unroll
      for (int i = 0; i < 5; ++i) *(LAS f32x4*)(smem + L_CS + w * 320 + i * 64 + (lane >> 4) * 16) = cs[i];
    }
  }
  bar_lds();

  float qn2 = 0.f;
  if (tid < 256) {
    const int b = tid >> 5, cc = tid & 31, tok = lane;
    const char LAS* pp = smem + L_PART + tok * 336 + w * 80;
    f32x4 cq = *(const LAS f32x4*)(smem + L_CS + (lane & 7) * 320 + w * 80 + (lane < 40 ? lane >> 3 : 0) * 16);
    const f32x2 st = *(const LAS f32x2*)(smem + L_STAT + tok * 8) + *(const LAS f32x2*)(smem + L_STAT + 512 + tok * 8);
#pragma unroll
    for (int e = 0; e < 4; ++e) { cq[e] += dpp_mov<0xB1>(cq[e]); cq[e] += dpp_mov<0x4E>(cq[e]); cq[e] += dpp_mov<0x141>(cq[e]); }
    const float mean = st[0] * (1.0f / D_);
    const float sd = ln_sd(fmaxf(fmaf(-mean, mean, st[1] * (1.0f / D_)), 0.f));
    const float mb = rbf(mean), sc = QSCALE / sd;
    if (w == 0) *(LAS unsigned*)(smem + L_STP + tok * 4) = pk2(mb, sd);
    unsigned qv[10];
#pragma unroll
    for (int j = 0; j < 5; ++j) {
      f32x4 a = *(const LAS f32x4*)(pp + j * 16) + *(const LAS f32x4*)(pp + 21504 + j * 16);
#pragma unroll
      for (int e = 0; e < 4; ++e) a[e] = fmaf(-mb, rdlane(cq[e], j * 8), a[e]) * sc;
      qv[2 * j] = pk2(a[0], a[1]); qv[2 * j + 1] = pk2(a[2], a[3]);
      qn2 = dot2(qv[2 * j], qv[2 * j], qn2); qn2 = dot2(qv[2 * j + 1], qv[2 * j + 1], qn2);
    }
    char LAS* qd = smem + L_Q + b * 1536;
    *(LAS u32x4*)(qd + cc * 16) = u32x4{qv[0], qv[1], qv[2], qv[3]};
    *(LAS u32x4*)(qd + (cc + 32) * 16) = u32x4{qv[4], qv[5], qv[6], qv[7]};
    *(LAS u32x2*)(qd + 1024 + cc * 8) = u32x2{qv[8], qv[9]};
  } else if (w == 5) {
    u32x4 gv[4];
#pragma unroll 1
    for (unsigned spin = 0; spin < SPIN_CAP; ++spin) {
      CFENCE();
      bool ok = true;
#pragma unroll
      for (int i = 0; i < 4; ++i) gv[i] = ld16(rs, lane * 16 + i * 1024, WS_FB);
#pragma unroll
      for (int i = 0; i < 4; ++i) ok &= (gv[i][0] == tag) & (gv[i][2] == tag);
      if (__all(ok)) break;
      __builtin_amdgcn_s_sleep(1);
    }
    float rm = __uint_as_float(gv[0][1]), kn = __uint_as_float(gv[0][3] & 0xffff0000u);
#pragma unroll
    for (int i = 1; i < 4; ++i) { rm = fmaxf(rm, __uint_as_float(gv[i][1])); kn = fmaxf(kn, __uint_as_float(gv[i][3] & 0xffff0000u)); }
    rm = max64(rm); kn = max64(kn);
    float neg = -4096.f;
#pragma unroll
    for (int i = 0; i < 4; ++i) neg = __uint_as_float(gv[i][1]) == rm ? fmaxf(neg, -(float)(gv[i][3] & 0xffffu)) : neg;
    neg = max64(neg);
    if (lane == 0) {
      *(LAS u32x4*)(smem + L_SUM) = u32x4{__float_as_uint(rm), (unsigned)(int)(-neg) & (S_ - 1), __float_as_uint(kn), tag};
      if (bid == 0) st4(rs, tag, WS_EP);
    }
  } else {
    const int z = (w == 4 ? 0 : w - 5) * 64 + lane;
#pragma unroll
    for (int i = 0; i < 16; ++i) *(LAS f32x4*)(smem + L_RING + i * 3072 + z * 16) = f32x4{0, 0, 0, 0};
  }
  bar_lds();

  KVRegs kv;
  const int k2o = (lane < 32 ? lane : 32) * 8, vo = (lane >> 5) * 352 + ((lane & 31) < 21 ? (lane & 31) : 21) * 16;
  auto load_tile = [&](int t) {
    kv.k1[t] = __builtin_bit_cast(bf16x8, ld16(rs, lane * 16, WS_KF1 + (w * 8 + t) * 1024));
    kv.k2[t] = __builtin_bit_cast(s16x4, ld8(rs, k2o, WS_KF2 + (w * 8 + t) * 272));
    kv.v[t][0] = __builtin_bit_cast(bf16x8, ld16(rs, vo, WS_VF + (w * 8 + t) * 1408));
    if (t < 7) kv.v[t][1] = __builtin_bit_cast(bf16x8, ld16(rs, vo + 704, WS_VF + (w * 8 + t) * 1408));
  };
  load_tile(0);
  kv.v[7][1] = __builtin_bit_cast(bf16x8, ld16(rs, vo + 704, WS_VF + (w * 8 + 7) * 1408));
  SB();
  const u32x4 sumv = *(const LAS u32x4*)(smem + L_SUM);
  const float rmax = __uint_as_float(__builtin_amdgcn_readfirstlane(sumv[0]));
  const int ridx = __builtin_amdgcn_readfirstlane(sumv[1]);
  tag = __builtin_amdgcn_readfirstlane(sumv[3]);
  if (tid < 256) {
    const float m = __builtin_amdgcn_sqrtf(qn2 * __uint_as_float(sumv[2])) * 1.01f;
    *(LAS u32x2*)(smem + L_Q + (tid >> 5) * 1536 + 1024 + ((tid & 31) + 32) * 8) = u32x2{pk2(-m, 0.f), 0u};
  }
  load_tile(1);
  load_tile(2);
  const unsigned stp0 = *(const LAS unsigned*)(smem + L_STP + (tid / 10) * 4);
  const unsigned stp1 = *(const LAS unsigned*)(smem + L_STP + (tid < 128 ? (512 + tid) / 10 : 63) * 4);
  unsigned xs[5];
  if (w == 6) {
#pragma unroll
    for (int i = 0; i < 5; ++i) xs[i] = *(const unsigned*)(x + (size_t)ridx * D_ + i * 128 + lane * 2);
  }
  bar_lds();

  const f32x16 zero16 = {0, 0, 0, 0, 0, 0, 0, 0, 0, 0, 0, 0, 0, 0, 0, 0};
  f32x16 oa = zero16, ob = zero16;
  bf16x8 q1 = *(const LAS bf16x8*)(smem + L_Q + w * 1536 + lane * 16);
  s16x4 q2 = *(const LAS s16x4*)(smem + L_Q + w * 1536 + 1024 + lane * 8);
  f32x16 sa = MFMA32(kv.k1[0], q1, zero16), sb;
  sa = MFMA32K8(kv.k2[0], q2, sa);
  u32x4 p1 = {0, 0, 0, 0};
  DmaJob dj;
  dj.on = 0; dj.sb = (const char*)wo; dj.sstr = 0; dj.lds = lds0;
  att_block(kv, smem, lane, ((w + 7) & 7) + 8, ((w + 1) & 7) + 8, (w + 1) & 7, q1, q2, sa, sb, p1, ob, oa,
            [&](int i) { if (i < 5) load_tile(3 + i); });
  if (w == 6) {
    float s = 0.f;
#pragma unroll
    for (int i = 0; i < 5; ++i) s = dot2(xs[i], 0x3f803f80u, s);
    const float mean = sum_halves(sum32(s)) * (1.0f / D_);
    f32x2 v2 = {0.f, 0.f};
#pragma unroll
    for (int i = 0; i < 5; ++i) { const f32x2 d = f32x2{bflo(xs[i]), bfhi(xs[i])} - mean; v2 += d * d; }
    const float sd = ln_sd(sum_halves(sum32(v2[0] + v2[1])) * (1.0f / D_));
    const float mb = rbf(mean), rc = 1.0f / sd;
#pragma unroll
    for (int i = 0; i < 5; ++i) *(LAS unsigned*)(smem + L_XS + i * 256 + lane * 4) = ln_pair(xs[i], mb, sd, rc);
  }
  if (w < 6) {
    const int ln = opaque(lane);
    const unsigned vo = w < 4 ? ln * (F_ * 2) : ln * 16;
#pragma unroll
    for (int i = 0; i < 5; ++i) dj.voff[i] = vo;
    dj.sstr = w < 4 ? 64 * F_ * 2 : 1024;
    dj.sb = (w < 4 ? (const char*)((w < 2 ? wg : wu) + fcol) : (const char*)(wd + (size_t)fcol * D_)) + (size_t)(w & 1) * 5 * dj.sstr;
    dj.lds = lds0 + L_G + w * 5120;
    dj.on = has_ffn;
  } else {
    w_voff(opaque(lane), dj.voff);
    dj.sb = (const char*)wo + hg * 160 + (size_t)(w - 6) * 40960; dj.lds = lds0 + L_W + (w - 6) * 5120; dj.on = 1;
  }
  att_block(kv, smem, lane, w, (w + 2) & 7, (w + 2) & 7, q1, q2, sa, sb, p1, oa, ob, [&](int i) { dma_hook(dj, 0, i); });

  w_voff(opaque(lane), dj.voff);
  dj.sstr = 0;
  u32x4 xe[2], ge[2];
  const bool sel_blk = (ridx >> 6) == qb;
#pragma unroll 1
  for (int attempt = 0;; ++attempt) {
#pragma unroll 1
    for (int st = attempt == 0 ? 2 : 0; st < 8; st += 2) {
      dj.on = 0;
      if (attempt == 0) {
        if (st == 2) {
          dj.sb = (const char*)wo + hg * 160 + (size_t)(2 + w) * 40960; dj.lds = lds0 + L_W + (2 + w) * 5120; dj.on = 1;
        } else if (st == 4) {
          if (has_ffn && w < 4) {
            const int ln = opaque(lane);
            const int g = ln >> 4, q = (ln & 15) >> 2, pp = ln & 3;
            const char LAS* gb = smem + L_G + (pp >> 1) * 10240 + (8 * g + q) * 16 + (pp & 1) * 8 + w * 2560;
            const char LAS* xs = smem + L_XS + w * 320 + g * 16;
            f32x4 a = {0, 0, 0, 0};
#pragma unroll
            for (int s = 0; s < 5; ++s)
              a = MFMA16(*(const LAS bf16x8*)(xs + s * 64), cat8(trread(gb + s * 512), trread(gb + s * 512 + 64)), a);
            if (ln < 16) *(LAS float*)(smem + L_FRED + ln * 16 + w * 4) = a[0];
          }
        } else {
          drain();
          dj.sb = (const char*)wo + hg * 160 + (size_t)(10 + w) * 40960; dj.lds = lds0 + L_WHI + w * 5120; dj.on = 1;
        }
      }
      att_block(kv, smem, lane, ((w + st + 7) & 7) + 8, ((w + st + 1) & 7) + 8, (w + st + 1) & 7, q1, q2, sa, sb, p1, ob, oa,
                [&](int i) { dma_hook(dj, 0, i); });
      dj.on = 0;
      if (attempt == 0) {
        if (st == 4) {
          if (has_ffn && w >= 4) {
            const int ln = opaque(lane);
            const int cl = ln < 40 ? ln : 39;
            const f32x4 g4 = *(const LAS f32x4*)(smem + L_FRED + (ln & 15) * 16);
            u32x2 wv2[8];
#pragma unroll
            for (int c = 0; c < 8; ++c) wv2[c] = *(const LAS u32x2*)(smem + L_DN + c * 1280 + (w - 4) * 320 + cl * 8);
            const float gu = rbf((g4[0] + g4[1]) + (g4[2] + g4[3]));
            const float up = dpp_mov<0x128>(gu);
            const float hval = rbf(rbf(silu_f(gu)) * up);
            f32x4 y = {0, 0, 0, 0};
#pragma unroll
            for (int c = 0; c < 8; ++c) {
              const float hc = rdlane(hval, c);
              y[0] = fmaf(hc, bflo(wv2[c][0]), y[0]); y[1] = fmaf(hc, bfhi(wv2[c][0]), y[1]);
              y[2] = fmaf(hc, bflo(wv2[c][1]), y[2]); y[3] = fmaf(hc, bfhi(wv2[c][1]), y[3]);
            }
            if (ln < 40) st16(rs, __builtin_bit_cast(u32x4, y), ln * 16 + (ln >= 20 ? 69120 - 320 : 0), WS_YP + (w - 4) * 138240 + fid * 320);
          }
        } else if (st == 6) {
          if (has_ffn && w == 2 && lane == 0) st4(rs, tag, WS_FY + fid * 4);
          dj.sb = (const char*)wo + hg * 160 + (size_t)(18 + w) * 40960; dj.lds = lds0 + L_WHI + (8 + w) * 5120; dj.on = w < 2;
        }
      }
      att_block(kv, smem, lane, (w + st) & 7, (w + st + (st == 6 ? 1 : 2)) & 7, (w + st + 2) & 7, q1, q2, sa, sb, p1, oa, ob,
                [&](int i) { dma_hook(dj, (attempt == 0) & (st == 2), i); });
    }
    __builtin_amdgcn_s_setprio(0);
    ob = MFMA32(kv.v[7][1], __builtin_bit_cast(bf16x8, p1), ob);
#pragma unroll
    for (int e = 0; e < 12; ++e) ob[e] += oa[e];

    const int tid4 = opaque(tid), ln4 = tid4 & 63;
    const float l = hi_half(ob[8]);
    const bool bad = attempt == 0 && __any(!(l >= 5.4e-20f && l <= 1e30f));
    LAS unsigned* ocnt = (LAS unsigned*)(smem + L_CNT);
    if (!bad) {
      const int b = (w + 7) & 7, hh = b >> 1, tok = qb * 64 + (b & 1) * 32 + (ln4 & 31), h4 = ln4 >> 5;
      const float inv = 1.0f / l;
      unsigned A0 = pk2(ob[0] * inv, ob[1] * inv), A1 = pk2(ob[2] * inv, ob[3] * inv);
      unsigned B0 = pk2(ob[4] * inv, ob[5] * inv), B1 = pk2(ob[6] * inv, ob[7] * inv);
      unsigned C0 = pk2(ob[8] * inv, ob[9] * inv), C1 = pk2(ob[10] * inv, ob[11] * inv);
      const int base = WS_O + ((tok >> 4) * 80 + hg * 10 + ((hh * 5) >> 1)) * 256 + (tok & 15) * 16;
      if ((hh & 1) == 0) {
        auto s0 = __builtin_amdgcn_permlane32_swap(A0, B0, false, false);
        auto s1 = __builtin_amdgcn_permlane32_swap(A1, B1, false, false);
        st16(rs, u32x4{s0[0], s1[0], s0[1], s1[1]}, base + h4 * 256);
        if (h4 == 0) st8(rs, u32x2{C0, C1}, base + 512);
      } else {
        auto s0 = __builtin_amdgcn_permlane32_swap(A0, C0, false, false);
        auto s1 = __builtin_amdgcn_permlane32_swap(A1, C1, false, false);
        st16(rs, h4 == 0 ? u32x4{s0[1], s1[1], B0, B1} : u32x4{B0, B1, s0[0], s1[0]}, base + 256 + h4 * 256);
        if (h4 == 0) st8(rs, u32x2{s0[0], s1[0]}, base + 8);
      }
    }
    drain();
    if (ln4 == 0 && lds_add_rtn(ocnt, bad ? 0x101u : 1u) == 7u && !bad) st4(rs, tag, WS_FC + qb * 128 + hg * 4);

#pragma unroll
    for (int i = 0; i < 2; ++i) {
      int n = i * 512 + tid4; n = n < 640 ? n : 639;
      const int row = n / 10, ch = n - row * 10, tok = qb * 64 + row;
      xe[i] = *(const u32x4*)(x + (size_t)tok * D_ + hg * 80 + ch * 8);
    }
    if (w == 7) {
#pragma unroll 1
      for (unsigned spin = 0; spin < SPIN_CAP; ++spin) {
        CFENCE();
        const unsigned v = ld4(rs, WS_FC + qb * 128 + (ln4 & 7) * 4);
        if (__all(v == tag) || lds_get(ocnt) > 0xffu) break;
        __builtin_amdgcn_s_sleep(1);
      }
    } else if (sel_blk && w >= 1) {
#pragma unroll 1
      for (unsigned spin = 0; spin < SPIN_CAP; ++spin) {
        CFENCE();
        const u32x4 v = ld16(rs, (ln4 < 54 ? ln4 : 53) * 16, WS_FY);
        if (__all((v[0] == tag) & (v[1] == tag) & (v[2] == tag) & (v[3] == tag))) break;
        __builtin_amdgcn_s_sleep(1);
      }
      const int lc = ln4 < 60 ? ln4 : 59, sub = lc / 20, c4 = lc - sub * 20;
      f32x4 t[12];
#pragma unroll
      for (int i = 0; i < 12; ++i)
        t[i] = __builtin_bit_cast(f32x4, ld16(rs, lc * 16 + i * 960, WS_YP + hg * 69120 + (w - 1) * 11520));
      f32x4 a = t[0];
#pragma unroll
      for (int i = 1; i < 12; ++i) a += t[i];
      if (ln4 < 60) *(LAS f32x4*)(smem + L_XSUM + ((w - 1) * 3 + sub) * 320 + c4 * 16) = a;
    }
    const bool selrow0 = qb * 64 + tid4 / 10 == ridx, selrow1 = qb * 64 + (512 + tid4) / 10 == ridx;
#pragma unroll
    for (int i = 0; i < 2; ++i) {
      const unsigned stp = i == 0 ? stp0 : stp1;
      const float mb = bflo(stp), sd = bfhi(stp), rc = 1.0f / sd;
#pragma unroll
      for (int e = 0; e < 4; ++e) ge[i][e] = ln_pair(xe[i][e], mb, sd, rc);
      if (!(i == 0 ? selrow0 : selrow1)) {
#pragma unroll
        for (int e = 0; e < 4; ++e) ge[i][e] = pk2(silu_f(bflo(ge[i][e])), silu_f(bfhi(ge[i][e])));
      }
    }
    bar_lds();
    if (lds_get(ocnt) <= 0xffu) break;
    {
      const int c32 = ln4 & 31, h = ln4 >> 5;
#pragma unroll 1
      for (int b = 0; b < 8; ++b) {
        const bf16x8 fq1 = *(const LAS bf16x8*)(smem + L_Q + b * 1536 + ln4 * 16);
        s16x4 fq2 = *(const LAS s16x4*)(smem + L_Q + b * 1536 + 1024 + ln4 * 8);
        if (h == 1) fq2 = s16x4{0, 0, 0, 0};
        float mx = -INFINITY;
#pragma unroll
        for (int t = 0; t < 8; ++t) {
          f32x16 s = MFMA32(kv.k1[t], fq1, zero16);
          s = MFMA32K8(kv.k2[t], fq2, s);
#pragma unroll
          for (int e = 0; e < 16; ++e) mx = fmaxf(mx, s[e]);
        }
        mx = fmaxf(mx, __shfl_xor(mx, 32));
        if (h == 0) *(LAS float*)(smem + L_MAXS + (w * 256 + b * 32 + c32) * 4) = mx;
      }
      bar_lds();
      if (tid4 < 256) {
        float mx = -INFINITY;
#pragma unroll
        for (int i = 0; i < 8; ++i) mx = fmaxf(mx, *(LAS float*)(smem + L_MAXS + (i * 256 + tid4) * 4));
        const float hi = rbf(mx), lo = mx - hi;
        *(LAS u32x2*)(smem + L_Q + (tid4 >> 5) * 1536 + 1024 + ((tid4 & 31) + 32) * 8) = u32x2{pk2(-hi, -lo), 0u};
      }
      bar_lds();
#pragma unroll
      for (int i = 0; i < 6; ++i) *(LAS f32x4*)(smem + L_RING + i * 8192 + tid4 * 16) = f32x4{0, 0, 0, 0};
      bar_lds();
      q1 = *(const LAS bf16x8*)(smem + L_Q + w * 1536 + lane * 16);
      q2 = *(const LAS s16x4*)(smem + L_Q + w * 1536 + 1024 + lane * 8);
      sa = MFMA32(kv.k1[0], q1, zero16);
      sa = MFMA32K8(kv.k2[0], q2, sa);
      p1 = u32x4{0, 0, 0, 0};
      oa = zero16; ob = zero16;
    }
  }

  const int tid5 = opaque(tid), ln5 = tid5 & 63;
#pragma unroll
  for (int s = 0; s < 5; ++s)
    af[s] = __builtin_bit_cast(bf16x8, ld16(rs, ln5 * 16 + s * 1024, WS_O + ((qb * 4 + mt) * 20 + kh * 10) * 1024));
  LAS float* ys = (LAS float*)(smem + L_YS);
  if (sel_blk && w == 7 && ln5 < 40) {
    f32x2 a = {0.f, 0.f};
#pragma unroll
    for (int i = 0; i < 18; ++i) a += *(const LAS f32x2*)(smem + L_XSUM + i * 320 + ln5 * 8);
    const float rsig = rbf(sigm_f(rbf(rmax)));
    *(LAS f32x2*)(ys + ln5 * 2) = f32x2{rbf(rbf(a[0]) * rsig), rbf(rbf(a[1]) * rsig)};
  }
  {
    f32x4 acc[5] = {{0, 0, 0, 0}, {0, 0, 0, 0}, {0, 0, 0, 0}, {0, 0, 0, 0}, {0, 0, 0, 0}};
    gemm_half(af, smem + (kh ? L_WHI : L_W), ln5, acc, [&](int t, const bf16x8 (&)[5]) {
      if (t < 5) af[t + 5] = __builtin_bit_cast(bf16x8, ld16(rs, ln5 * 16 + (t + 5) * 1024, WS_O + ((qb * 4 + mt) * 20 + kh * 10) * 1024));
    });
    write_part(smem + L_PART2, kh, mt, ln5, acc);
  }

  bar_lds();
#pragma unroll
  for (int i = 0; i < 2; ++i) {
    const int n = i * 512 + tid5;
    if (n < 640) {
      const int row = n / 10, ch = n - row * 10, tok = qb * 64 + row;
      const char LAS* pp = smem + L_PART2 + row * 336 + ch * 32;
      const f32x4 s0 = *(const LAS f32x4*)(pp) + *(const LAS f32x4*)(pp + 21504);
      const f32x4 s1 = *(const LAS f32x4*)(pp + 16) + *(const LAS f32x4*)(pp + 21504 + 16);
      if (tok == ridx) {
#pragma unroll
        for (int e = 0; e < 4; ++e)
          ge[i][e] = pk2(silu_f(rbf(bflo(ge[i][e]) + ys[ch * 8 + 2 * e])), silu_f(rbf(bfhi(ge[i][e]) + ys[ch * 8 + 2 * e + 1])));
      }
      u32x4 r;
      r[0] = pk2(bflo(xe[i][0]) + rbf(rbf(s0[0]) * bflo(ge[i][0])), bfhi(xe[i][0]) + rbf(rbf(s0[1]) * bfhi(ge[i][0])));
      r[1] = pk2(bflo(xe[i][1]) + rbf(rbf(s0[2]) * bflo(ge[i][1])), bfhi(xe[i][1]) + rbf(rbf(s0[3]) * bfhi(ge[i][1])));
      r[2] = pk2(bflo(xe[i][2]) + rbf(rbf(s1[0]) * bflo(ge[i][2])), bfhi(xe[i][2]) + rbf(rbf(s1[1]) * bfhi(ge[i][2])));
      r[3] = pk2(bflo(xe[i][3]) + rbf(rbf(s1[2]) * bflo(ge[i][3])), bfhi(xe[i][3]) + rbf(rbf(s1[3]) * bfhi(ge[i][3])));
      *(u32x4*)(out + (size_t)tok * D_ + hg * 80 + ch * 8) = r;
    }
  }


}

extern "C" void kernel_launch(void* const* d_in, const int* in_sizes, int n_in, void* d_out, int out_size,
                              void* d_ws, size_t ws_size, hipStream_t stream) {
  fused<<<256, 512, 0, stream>>>((const u16*)d_in[0], (const u16*)d_in[1], (const u16*)d_in[2], (const u16*)d_in[3],
                                 (const u16*)d_in[4], (const u16*)d_in[6], (const u16*)d_in[7], (const u16*)d_in[8],
                                 (const u16*)d_in[9], (char*)d_ws, (u16*)d_out);
}
```

```cpp
#include <hip/hip_runtime.h>
#include <hip/hip_bf16.h>
#include <stdint.h>

typedef __attribute__((ext_vector_type(8))) __bf16 bf16x8;
typedef __attribute__((ext_vector_type(2))) __bf16 bf16x2;
typedef __attribute__((ext_vector_type(4))) short s16x4;
typedef __attribute__((ext_vector_type(2))) float f32x2;
typedef __attribute__((ext_vector_type(4))) float f32x4;
typedef __attribute__((ext_vector_type(16))) float f32x16;
typedef __attribute__((ext_vector_type(2))) unsigned u32x2;
typedef __attribute__((ext_vector_type(4))) unsigned u32x4;
typedef unsigned short u16;
#define LAS __attribute__((address_space(3)))
#define DI __device__ __forceinline__
#define SB() __builtin_amdgcn_sched_barrier(0)
#define CFENCE() asm volatile("" ::: "memory")

constexpr int S_ = 2048, D_ = 640, F_ = 1728;

constexpr int WS_O = 0;
constexpr int WS_KF1 = WS_O + S_ * D_ * 2;
constexpr int WS_KF2 = WS_KF1 + 65536;
constexpr int WS_VF = WS_KF2 + 64 * 272;
constexpr int WS_YP = WS_VF + 64 * 1408;
constexpr int WS_YQ = WS_YP + 216 * D_ * 4;
constexpr int WS_FC = WS_YQ + 216 * 320 + 128 * 4;
constexpr int WS_FB = WS_FC + 32768;
constexpr int WS_FY = WS_FB + 32768;
constexpr int WS_FZ = WS_FY + 32768;
constexpr int WS_EP = WS_FZ + 32768;
static_assert(WS_FC % 128 == 0, "flag lines");
constexpr int WS_BYTES = WS_EP + 256;

constexpr int L_W = 0;
constexpr int L_SCR = 101376;
constexpr int L_WK = 102400, L_WV = 128000, L_X = 153600;
constexpr int L_WHI = 112640;
constexpr int L_RING = 51200;
constexpr int L_Q = 100352;
constexpr int L_PART = 112640;
constexpr int L_G = 112640, L_U = 122880, L_DN = 133120;
constexpr int L_FRED = 143360;
constexpr int L_XS = 161280;
constexpr int L_SUM = 162560;
constexpr int L_STAT = 155648;
constexpr int L_CS = 156672;
constexpr int L_STP = 159232;
constexpr int L_MAXS = L_RING;
constexpr int L_PART2 = L_RING;
constexpr int L_XSUM = L_RING + 43008;
constexpr int L_YS = L_RING + 48768;
constexpr int L_CNT = L_Q + 1024 + 256 + 4;
constexpr int LDS_BYTES = 163840;

constexpr float QSCALE = 0.22360679774997896f * 1.4426950408889634f;
constexpr unsigned SPIN_CAP = 1u << 18;
#define POLL_SLEEP 4

DI float bflo(unsigned v) { return __uint_as_float(v << 16); }
DI float bfhi(unsigned v) { return __uint_as_float(v & 0xffff0000u); }
DI unsigned pk2(float a, float b) {
  f32x2 t = {a, b};
  return __builtin_bit_cast(unsigned, __builtin_convertvector(t, bf16x2));
}
DI float rbf(float a) { return bflo(pk2(a, a)); }
DI float dot2(unsigned a, unsigned b, float c) {
  return __builtin_amdgcn_fdot2_f32_bf16(__builtin_bit_cast(bf16x2, a), __builtin_bit_cast(bf16x2, b), c, false);
}
DI s16x4 trread(const char LAS* p) {
  return __builtin_bit_cast(s16x4, __builtin_amdgcn_ds_read_tr16_b64_v4i16((LAS s16x4*)p));
}
DI bf16x8 cat8(s16x4 a, s16x4 b) {
  return __builtin_bit_cast(bf16x8, __builtin_shufflevector(a, b, 0, 1, 2, 3, 4, 5, 6, 7));
}
#define MFMA16(a, b, c) __builtin_amdgcn_mfma_f32_16x16x32_bf16((a), (b), (c), 0, 0, 0)
#define MFMA32(a, b, c) __builtin_amdgcn_mfma_f32_32x32x16_bf16((a), (b), (c), 0, 0, 0)
#define MFMA32K8(a, b, c) __builtin_amdgcn_mfma_f32_32x32x8bf16_1k((a), (b), (c), 0, 0, 0)
#define EXP2(x) __builtin_amdgcn_exp2f(x)
DI float sigm_f(float v) { return __builtin_amdgcn_rcpf(1.0f + EXP2(v * -1.4426950408889634f)); }
DI float silu_f(float v) { return v * sigm_f(v); }
DI void bar_lds() { asm volatile("s_waitcnt lgkmcnt(0)\n\ts_barrier" ::: "memory"); }
DI void bar_all() { asm volatile("s_waitcnt vmcnt(0) lgkmcnt(0)\n\ts_barrier" ::: "memory"); }
DI void drain() { asm volatile("s_waitcnt vmcnt(0)" ::: "memory"); }
DI void lds_add(LAS unsigned* p, unsigned v) {
  asm volatile("s_waitcnt lgkmcnt(0)" ::: "memory");
  __hip_atomic_fetch_add(p, v, __ATOMIC_RELAXED, __HIP_MEMORY_SCOPE_WORKGROUP);
}
DI unsigned lds_add_rtn(LAS unsigned* p, unsigned v) {
  asm volatile("s_waitcnt lgkmcnt(0)" ::: "memory");
  const unsigned r = __hip_atomic_fetch_add(p, v, __ATOMIC_RELAXED, __HIP_MEMORY_SCOPE_WORKGROUP);
  CFENCE();
  return r;
}
DI unsigned lds_get(LAS unsigned* p) {
  CFENCE();
  const unsigned r = __builtin_amdgcn_readfirstlane(__hip_atomic_load(p, __ATOMIC_RELAXED, __HIP_MEMORY_SCOPE_WORKGROUP));
  CFENCE();
  return r;
}
DI void lds_wait(LAS unsigned* p, unsigned target) {
#pragma unroll 1
  while (lds_get(p) < target) __builtin_amdgcn_s_sleep(1);
}
DI void dma16(const void* sbase, unsigned voff, unsigned lds) {
  unsigned keep;
  asm volatile("s_mov_b32 %0, m0\n\ts_mov_b32 m0, %3\n\ts_nop 0\n\tglobal_load_lds_dwordx4 %1, %2\n\ts_mov_b32 m0, %0"
               : "=&s"(keep) : "v"(voff), "s"(sbase), "s"(lds) : "memory");
}
DI void dma16_if(unsigned on, const void* sbase, unsigned voff, unsigned lds) {
  unsigned keep;
  on = __builtin_amdgcn_readfirstlane(on);
  lds = __builtin_amdgcn_readfirstlane(lds);
  sbase = (const void*)(((unsigned long long)__builtin_amdgcn_readfirstlane((unsigned)((unsigned long long)sbase >> 32)) << 32) |
                        (unsigned)__builtin_amdgcn_readfirstlane((unsigned)(unsigned long long)sbase));
  asm volatile("s_cmp_eq_u32 %4, 0\n\ts_cbranch_scc1 1f\n\ts_mov_b32 %0, m0\n\ts_mov_b32 m0, %3\n\ts_nop 1\n\t"
               "global_load_lds_dwordx4 %1, %2\n\ts_mov_b32 m0, %0\n1:"
               : "=&s"(keep) : "v"(voff), "s"(sbase), "s"(lds), "s"(on) : "memory", "scc");
}
DI void touch4(const void* sbase, unsigned voff, unsigned lds) {
  unsigned keep;
  asm volatile("s_mov_b32 %0, m0\n\ts_mov_b32 m0, %3\n\ts_nop 1\n\tglobal_load_lds_dword %1, %2\n\ts_mov_b32 m0, %0"
               : "=&s"(keep) : "v"(voff), "s"(sbase), "s"(lds) : "memory");
}
typedef __amdgpu_buffer_rsrc_t rsrc_t;
DI u32x4 ld16(rsrc_t r, int v, int s = 0) { return __builtin_amdgcn_raw_buffer_load_b128(r, v, s, 16); }
DI u32x2 ld8(rsrc_t r, int v, int s = 0) { return __builtin_amdgcn_raw_buffer_load_b64(r, v, s, 16); }
DI unsigned ld4(rsrc_t r, int v, int s = 0) { return __builtin_amdgcn_raw_buffer_load_b32(r, v, s, 16); }
DI void st16(rsrc_t r, u32x4 d, int v, int s = 0) { __builtin_amdgcn_raw_buffer_store_b128(d, r, v, s, 16); }
DI void st8(rsrc_t r, u32x2 d, int v, int s = 0) { __builtin_amdgcn_raw_buffer_store_b64(d, r, v, s, 16); }
DI void st4(rsrc_t r, unsigned d, int v, int s = 0) { __builtin_amdgcn_raw_buffer_store_b32(d, r, v, s, 16); }

template <int CTRL> DI float dpp_mov(float v) {
  return __builtin_bit_cast(float, __builtin_amdgcn_update_dpp(0, __builtin_bit_cast(int, v), CTRL, 0xf, 0xf, true));
}
DI float sum16(float v) {
  v += dpp_mov<0xB1>(v); v += dpp_mov<0x4E>(v); v += dpp_mov<0x141>(v); v += dpp_mov<0x140>(v);
  return v;
}
DI float sum_rows(float v) {
  const unsigned b = __float_as_uint(v);
  auto r = __builtin_amdgcn_permlane16_swap(b, b, false, false);
  return __uint_as_float(r[0]) + __uint_as_float(r[1]);
}
DI float sum_halves(float v) {
  const unsigned b = __float_as_uint(v);
  auto r = __builtin_amdgcn_permlane32_swap(b, b, false, false);
  return __uint_as_float(r[0]) + __uint_as_float(r[1]);
}
DI float hi_half(float v) {
  const unsigned b = __float_as_uint(v);
  auto r = __builtin_amdgcn_permlane32_swap(b, b, false, false);
  return __uint_as_float(r[1]);
}
DI float max64(float v) {
  v = fmaxf(v, dpp_mov<0xB1>(v)); v = fmaxf(v, dpp_mov<0x4E>(v)); v = fmaxf(v, dpp_mov<0x141>(v)); v = fmaxf(v, dpp_mov<0x140>(v));
  auto r = __builtin_amdgcn_permlane16_swap(__float_as_uint(v), __float_as_uint(v), false, false);
  v = fmaxf(__uint_as_float(r[0]), __uint_as_float(r[1]));
  auto q = __builtin_amdgcn_permlane32_swap(__float_as_uint(v), __float_as_uint(v), false, false);
  return fmaxf(__uint_as_float(q[0]), __uint_as_float(q[1]));
}
DI int opaque(int v) { asm volatile("" : "+v"(v)); return v; }
DI float sum32(float v) { return sum_rows(sum16(v)); }
DI float rdlane(float v, int l) { return __builtin_bit_cast(float, __builtin_amdgcn_readlane(__builtin_bit_cast(int, v), l)); }

DI float ln_sd(float var) { return rbf(sqrtf(rbf(rbf(var) + 1e-5f))); }
DI unsigned ln_pair(unsigned xp, float mb, float sd, float rc) {
  const unsigned dp = pk2(bflo(xp) - mb, bfhi(xp) - mb);
  const f32x2 d = {bflo(dp), bfhi(dp)};
  const f32x2 q0 = d * rc;
  const f32x2 a = __builtin_elementwise_fma(__builtin_elementwise_fma(-q0, f32x2{sd, sd}, d), f32x2{rc, rc}, q0);
  return pk2(a[0], a[1]);
}
DI void load_piece(const u16* __restrict__ xrow0, int lane, u32x4 (&xp)[10]) {
#pragma unroll
  for (int i = 0; i < 10; ++i) {
    const int n = i * 64 + lane, row = n / 40, ch = n - row * 40;
    xp[i] = *(const u32x4*)((const char*)xrow0 + row * 1280 + ch * 16);
  }
}
DI void bounce_piece(char LAS* blk, int lane, const u32x4 (&xp)[10], bf16x8 (&af)[10]) {
#pragma unroll
  for (int i = 0; i < 10; ++i) {
    const int n = i * 64 + lane, row = n / 40, ch = n - row * 40;
    *(LAS u32x4*)(blk + row * 656 + ch * 16) = xp[i];
  }
#pragma unroll
  for (int s = 0; s < 10; ++s) af[s] = *(const LAS bf16x8*)(blk + (lane & 15) * 656 + (lane >> 4) * 16 + s * 64);
}
DI void w_voff(int lane, unsigned (&voff)[5]) {
#pragma unroll
  for (int i = 0; i < 5; ++i) {
    const int n = i * 64 + lane, p = n / 10, ch = n - p * 10;
    const int k = (p & ~12) | ((p & 4) << 1) | ((p & 8) >> 1);
    voff[i] = k * 1280 + ch * 16;
  }
}
template <class F>
DI void gemm_half(bf16x8 (&af)[10], const char LAS* wimg, int lane, f32x4 (&acc)[5], F side) {
  const int g = lane >> 4, q = (lane & 15) >> 2, pp = lane & 3;
  const char LAS* wb = wimg + (16 * (g >> 1) + 4 * (g & 1) + q) * 160 + 8 * pp;
  bf16x8 wf[3][5];
#pragma unroll
  for (int p = 0; p < 2; ++p)
#pragma unroll
    for (int i = 0; i < 5; ++i) wf[p][i] = cat8(trread(wb + p * 5120 + i * 32), trread(wb + p * 5120 + 1280 + i * 32));
#pragma unroll
  for (int s = 0; s < 10; ++s) {
#pragma unroll
    for (int i = 0; i < 5; ++i) {
      acc[i] = MFMA16(wf[s % 3][i], af[s], acc[i]);
      SB();
      if (s < 8) wf[(s + 2) % 3][i] = cat8(trread(wb + (s + 2) * 5120 + i * 32), trread(wb + (s + 2) * 5120 + 1280 + i * 32));
      SB();
    }
    side(s, wf[s % 3]);
    SB();
  }
}
DI void write_part(char LAS* part, int kh, int mt, int lane, const f32x4 (&acc)[5]) {
  char LAS* p = part + kh * 21504 + (mt * 16 + (lane & 15)) * 336 + (lane >> 4) * 16;
#pragma unroll
  for (int i = 0; i < 5; ++i) *(LAS f32x4*)(p + i * 64) = acc[i];
}

struct KVRegs { bf16x8 k1[8]; s16x4 k2[8]; bf16x8 v[8][2]; };
struct DmaJob { const char* sb; unsigned sstr, lds, on; unsigned voff[5]; };

#define EXP_PK(dst, S, b) { dst[0] = pk2(EXP2(S[b]), EXP2(S[b + 1])); dst[1] = pk2(EXP2(S[b + 2]), EXP2(S[b + 3])); }
template <bool NEWBLK, int PRIO>
DI void att_step(f32x16& sc, f32x16& sn, const bf16x8& k1n, const s16x4& k2n, const bf16x8& q1n, const s16x4& q2n,
                 const bf16x8& v0, const bf16x8& v1prev, u32x4& p1, f32x16& oprev, f32x16& ocur) {
  const f32x16 zero16 = {0, 0, 0, 0, 0, 0, 0, 0, 0, 0, 0, 0, 0, 0, 0, 0};
  u32x2 a, b, c, d;
  if (PRIO >= 0) __builtin_amdgcn_s_setprio(PRIO);
  sn = MFMA32(k1n, q1n, zero16); SB();
  EXP_PK(a, sc, 0); SB();
  sn = MFMA32K8(k2n, q2n, sn); SB();
  EXP_PK(b, sc, 4); SB();
  oprev = MFMA32(v1prev, __builtin_bit_cast(bf16x8, p1), oprev); SB();
  EXP_PK(c, sc, 8); SB();
  const u32x4 p0 = {a[0], a[1], b[0], b[1]};
  if (NEWBLK) ocur = MFMA32(v0, __builtin_bit_cast(bf16x8, p0), ocur);
  else ocur = MFMA32(v0, __builtin_bit_cast(bf16x8, p0), oprev);
  SB();
  EXP_PK(d, sc, 12); SB();
  p1 = u32x4{c[0], c[1], d[0], d[1]};
}
template <class H>
DI void att_block(const KVRegs& kv, char LAS* smem, int lane, int sout, int sin, int bnext, bf16x8& q1, s16x4& q2,
                  f32x16& sa, f32x16& sb, u32x4& p1, f32x16& oprev, f32x16& ocur, H hook) {
  LAS f32x4* so = (LAS f32x4*)(smem + L_RING + sout * 3072) + lane;
  LAS f32x4* si = (LAS f32x4*)(smem + L_RING + sin * 3072) + lane;
  att_step<true, -1>(sa, sb, kv.k1[1], kv.k2[1], q1, q2, kv.v[0][0], kv.v[7][1], p1, oprev, ocur);
  hook(0); SB();
  att_step<false, 1>(sb, sa, kv.k1[2], kv.k2[2], q1, q2, kv.v[1][0], kv.v[0][1], p1, ocur, ocur);
  so[0] = f32x4{oprev[0], oprev[1], oprev[2], oprev[3]};
  hook(1); SB();
  att_step<false, -1>(sa, sb, kv.k1[3], kv.k2[3], q1, q2, kv.v[2][0], kv.v[1][1], p1, ocur, ocur);
  so[64] = f32x4{oprev[4], oprev[5], oprev[6], oprev[7]};
  hook(2); SB();
  att_step<false, 0>(sb, sa, kv.k1[4], kv.k2[4], q1, q2, kv.v[3][0], kv.v[2][1], p1, ocur, ocur);
  so[128] = f32x4{oprev[8], oprev[9], oprev[10], oprev[11]};
  hook(3); SB();
  att_step<false, -1>(sa, sb, kv.k1[5], kv.k2[5], q1, q2, kv.v[4][0], kv.v[3][1], p1, ocur, ocur);
  hook(4);
  bar_lds();
  const bf16x8 q1n = *(const LAS bf16x8*)(smem + L_Q + bnext * 1536 + lane * 16);
  const s16x4 q2n = *(const LAS s16x4*)(smem + L_Q + bnext * 1536 + 1024 + lane * 8);
  SB();
  att_step<false, 3>(sb, sa, kv.k1[6], kv.k2[6], q1, q2, kv.v[5][0], kv.v[4][1], p1, ocur, ocur);
  {
    const f32x4 i0 = si[0], i1 = si[64], i2 = si[128];
#pragma unroll
    for (int e = 0; e < 4; ++e) { oprev[e] = i0[e]; oprev[4 + e] = i1[e]; oprev[8 + e] = i2[e]; }
  }
  hook(5); SB();
  att_step<false, -1>(sa, sb, kv.k1[7], kv.k2[7], q1, q2, kv.v[6][0], kv.v[5][1], p1, ocur, ocur);
  hook(6); SB();
  att_step<false, 2>(sb, sa, kv.k1[0], kv.k2[0], q1n, q2n, kv.v[7][0], kv.v[6][1], p1, ocur, ocur);
  q1 = q1n; q2 = q2n;
}
DI void dma_hook(const DmaJob& dj, unsigned waitmode, int i) {
  if (i == 4) {
    asm volatile("s_cmp_eq_u32 %0, 0\n\ts_cbranch_scc1 1f\n\ts_waitcnt vmcnt(3)\n\ts_cmp_eq_u32 %0, 1\n\ts_cbranch_scc1 1f\n\t"
                 "s_waitcnt vmcnt(0)\n1:" :: "s"(__builtin_amdgcn_readfirstlane(waitmode)) : "memory", "scc");
  } else if (i != 1) {
    const int c = i == 0 ? 0 : (i < 4 ? i - 1 : i - 2);
    dma16_if(dj.on, dj.sb + (size_t)c * dj.sstr, dj.voff[c], dj.lds + c * 1024);
  }
}
DI void poll_words(rsrc_t rs, int voff, int soff, unsigned tag) {
#pragma unroll 1
  for (unsigned spin = 0; spin < SPIN_CAP; ++spin) {
    CFENCE();
    const unsigned v = ld4(rs, voff, soff);
    if (__all(v == tag)) break;
    __builtin_amdgcn_s_sleep(1);
  }
  CFENCE();
}

__global__ __launch_bounds__(512, 2) void fused(const u16* __restrict__ x, const u16* __restrict__ wq,
                                                const u16* __restrict__ wk, const u16* __restrict__ wv,
                                                const u16* __restrict__ wo, const u16* __restrict__ wr,
                                                const u16* __restrict__ wg, const u16* __restrict__ wu,
                                                const u16* __restrict__ wd, char* ws, u16* __restrict__ out) {
  __shared__ __attribute__((aligned(16))) char smem_[LDS_BYTES];
  char LAS* smem = (char LAS*)smem_;
  const unsigned lds0 = (unsigned)(size_t)smem;
  const int tid = threadIdx.x, lane = tid & 63;
  const int w = __builtin_amdgcn_readfirstlane(tid >> 6);
  const int bid = blockIdx.x, xcd = bid & 7, jj = bid >> 3;
  const int qb = xcd * 4 + (jj >> 3), hg = jj & 7, pid = qb * 8 + hg;
  const bool has_ffn = qb < 27;
  const int fcol = pid * 8;
  const int tok0 = pid * 8, tile = tok0 >> 5;
  const rsrc_t rs = __builtin_amdgcn_make_buffer_rsrc(ws, 0, WS_BYTES, 0x00020000);
  LAS float* scr = (LAS float*)(smem + L_SCR);
  LAS unsigned* cnt = (LAS unsigned*)(smem + L_SCR + 96);

  unsigned tag = 0;
  u32x4 late = {0, 0, 0, 0};
  const int mt = w & 3, kh = w >> 2;
  bf16x8 af[10];
  const u16* xpiece = x + (size_t)(qb * 64 + mt * 16) * D_ + kh * 320;
  if (w < 4) {
    const int half = lane >> 5, l32 = lane & 31, tl = 2 * w + half;
    u32x2 xv[5], wv5[5];
    u32x4 xp[10];
#pragma unroll
    for (int i = 0; i < 5; ++i) xv[i] = *(const u32x2*)(x + (size_t)(tok0 + tl) * D_ + i * 128 + l32 * 4);
    const unsigned ep = ld4(rs, WS_EP);
#pragma unroll
    for (int i = 0; i < 5; ++i) wv5[i] = *(const u32x2*)(wr + i * 128 + l32 * 4);
    if (tid < 2) *((LAS u32x4*)cnt + tid) = u32x4{0, 0, 0, 0};
    bar_lds();
    load_piece(xpiece, lane, xp);
    SB();
    float s = 0.f;
#pragma unroll
    for (int i = 0; i < 5; ++i) { s = dot2(xv[i][0], 0x3f803f80u, s); s = dot2(xv[i][1], 0x3f803f80u, s); }

    const float mean = sum32(s) * (1.0f / D_);
    f32x2 v2 = {0.f, 0.f};
#pragma unroll
    for (int i = 0; i < 10; ++i) {
      const f32x2 d = f32x2{bflo(xv[i >> 1][i & 1]), bfhi(xv[i >> 1][i & 1])} - mean;
      v2 += d * d;
    }
    const float sd = ln_sd(sum32(v2[0] + v2[1]) * (1.0f / D_));
    const float mb = rbf(mean), rc = 1.0f / sd;
#pragma unroll
    for (int i = 0; i < 5; ++i) {
      xv[i] = u32x2{ln_pair(xv[i][0], mb, sd, rc), ln_pair(xv[i][1], mb, sd, rc)};
      *(LAS u32x2*)(smem + L_X + tl * 1280 + (((i * 16 + (l32 >> 1)) ^ tl) * 16) + (l32 & 1) * 8) = xv[i];
    }
    if (lane == 0) lds_add(cnt + 0, 1);
    float racc = 0.f;
#pragma unroll
    for (int i = 0; i < 5; ++i) { racc = dot2(xv[i][0], wv5[i][0], racc); racc = dot2(xv[i][1], wv5[i][1], racc); }
    racc = sum32(racc);
    if (l32 == 0) scr[tl] = racc;
    tag = __builtin_amdgcn_readfirstlane(ep) + 1u;
    lds_wait(cnt + 0, 8);

    const int g = lane >> 4, c = lane & 15, q = c >> 2, pp = lane & 3, r8 = c & 7;
    const int mat = w >> 1, nt = w & 1;
    const char LAS* wb = smem + L_WK + mat * 25600 + (8 * g + q) * 40 + nt * 32 + 8 * pp;
    const char LAS* xb0 = smem + L_X + r8 * 1280 + (g ^ r8) * 16;
    const char LAS* xb1 = smem + L_X + r8 * 1280 + ((4 + g) ^ r8) * 16;
    bf16x8 wf[20], xf[20];
#pragma unroll
    for (int s2 = 0; s2 < 20; ++s2) {
      xf[s2] = *(const LAS bf16x8*)(((s2 & 1) ? xb1 : xb0) + (s2 >> 1) * 128);
      wf[s2] = cat8(trread(wb + s2 * 1280), trread(wb + s2 * 1280 + 160));
    }
    f32x4 a0 = {0, 0, 0, 0}, a1 = {0, 0, 0, 0};
    if (mat == 0) {
#pragma unroll
      for (int s2 = 0; s2 < 20; s2 += 2) { a0 = MFMA16(wf[s2], xf[s2], a0); a1 = MFMA16(wf[s2 + 1], xf[s2 + 1], a1); }
    } else {
#pragma unroll
      for (int s2 = 0; s2 < 20; s2 += 2) { a0 = MFMA16(xf[s2], wf[s2], a0); a1 = MFMA16(xf[s2 + 1], wf[s2 + 1], a1); }
    }
    if (lane == 0) lds_add(cnt + 2, 1);
    a0 += a1;
    u32x2 pk = {pk2(a0[0], a0[1]), pk2(a0[2], a0[3])};
    int soff; bool son;
    if (mat == 0) {
      float n2 = dot2(pk[0], pk[0], dot2(pk[1], pk[1], 0.f));
      const int r = (tok0 & 31) + c;
      if (nt == 0) {
        soff = WS_KF1 + tile * 1024 + (r + 32 * (g >> 1)) * 16 + (g & 1) * 8; son = c < 8;
        n2 = sum_halves(sum_rows(n2));
      } else {
        if (lane == 8) pk = u32x2{0x3f803f80u, 0u};
        soff = WS_KF2 + tile * 272 + (lane < 8 ? r : 32) * 8; son = lane <= 8;
      }
      if (lane < 8) scr[8 + nt * 8 + lane] = n2;
    } else {
      const int d = 16 * nt + c, u = (tok0 & 31) >> 3;
      if (d == 20) pk = u32x2{0x3f803f80u, 0x3f803f80u};
      if (d == 21) pk = u32x2{0u, 0u};
      soff = WS_VF + ((tile * 2 + (u >> 1)) * 2 + g) * 352 + d * 16 + (u & 1) * 8; son = g < 2 && d < 22;
    }
    if (son) st8(rs, pk, soff);
    lds_wait(cnt + 2, 4);
    bounce_piece(smem + L_WK + w * 10496, lane, xp, af);
    drain();
    if (lane == 0 && lds_add_rtn(cnt + 3, 1) == 3) {
      f32x4 sv[6];
#pragma unroll
      for (int i = 0; i < 6; ++i) sv[i] = *(const LAS f32x4*)(scr + 4 * i);
      float rm = sv[0][0], km = sv[2][0] + sv[4][0]; int ri = 0;
#pragma unroll
      for (int i = 1; i < 8; ++i) {
        const float rr = sv[i >> 2][i & 3];
        const bool t = rr > rm;
        rm = t ? rr : rm; ri = t ? i : ri;
        km = fmaxf(km, sv[2 + (i >> 2)][i & 3] + sv[4 + (i >> 2)][i & 3]);
      }
      const unsigned kb = (__float_as_uint(km) + 0xffffu) & 0xffff0000u;
      st16(rs, u32x4{tag, __float_as_uint(rm), tag, kb | (unsigned)(tok0 + ri)}, WS_FB + pid * 128);
    }

    lds_wait(cnt + 4, 4);
    {
      unsigned voff[5];
      w_voff(lane, voff);
#pragma unroll 1
      for (int G = w; G < 8; G += 4) {
        const char* sb = (const char*)wq + hg * 160 + (size_t)G * 40960;
#pragma unroll
        for (int c = 0; c < 5; ++c) dma16(sb, voff[c], lds0 + L_W + G * 5120 + c * 1024);
      }
    }
  } else {
    u32x4 xp[10], a[14];
    const int t2 = tid - 256;
    const unsigned ep = ld4(rs, WS_EP);
    const int rot6 = (jj + xcd) % 6;
    const int tl16 = (t2 < 64 ? t2 : 63) * 16 + 24576;
#pragma unroll
    for (int i = 0; i < 6; ++i) {
      const int j = (i + rot6 >= 6 ? i + rot6 - 6 : i + rot6) * 4096;
      a[i] = *(const u32x4*)((const char*)wk + j + t2 * 16);
      a[7 + i] = *(const u32x4*)((const char*)wv + j + t2 * 16);
      if (i == 0) bar_lds();
    }
    a[6] = *(const u32x4*)((const char*)wk + tl16);
    a[13] = *(const u32x4*)((const char*)wv + tl16);
    if (w == 7) {
      const char* pc = (const char*)__builtin_amdgcn_s_getpc();
      touch4(pc + 4096 + (jj % 3) * 10240, lane * 128, lds0 + L_SCR + 512);
      touch4(pc + 4096 + (jj % 3) * 10240 + 8192, (lane & 15) * 128, lds0 + L_SCR + 512);
    }
    unsigned voff[5];
    w_voff(lane, voff);
    SB();
#pragma unroll
    for (int i = 0; i < 6; ++i) {
      const int j = (i + rot6 >= 6 ? i + rot6 - 6 : i + rot6) * 4096;
      *(LAS u32x4*)(smem + L_WK + j + t2 * 16) = a[i];
      *(LAS u32x4*)(smem + L_WV + j + t2 * 16) = a[7 + i];
    }
    *(LAS u32x4*)(smem + L_WK + tl16) = a[6];
    *(LAS u32x4*)(smem + L_WV + tl16) = a[13];
    tag = __builtin_amdgcn_readfirstlane(ep) + 1u;
    asm volatile("" :: "s"(tag) : "memory");
    if (lane == 0) lds_add(cnt + 0, 1);

    load_piece(xpiece, lane, xp);
    late = *(const u32x4*)((const char*)wq + hg * 160 + 19 * 40960 + voff[4]);
    bounce_piece(smem + L_W + (w - 4) * 10496, lane, xp, af);
#pragma unroll
    for (int s = 0; s < 10; ++s) asm volatile("" : "+v"(af[s]));
    asm volatile("" : "+v"(late));
    if (lane == 0) lds_add(cnt + 4, 1);
    lds_wait(cnt + 4, 4);

    int G = ((jj >> 3) * 3 + (w - 4)) % 12;
#pragma unroll 1
    for (int i = 0; i < 3; ++i) {
      const char* sb = (const char*)wq + hg * 160 + (size_t)(8 + G) * 40960;
#pragma unroll
      for (int c = 0; c < 5; ++c) dma16_if(G * 5 + c != 59, sb, voff[c], lds0 + L_W + (8 + G) * 5120 + c * 1024);
      G = G >= 8 ? G - 8 : G + 4;
    }

  }
  float s1 = 0.f, s2q = 0.f;
#pragma unroll
  for (int s = 0; s < 10; ++s) {
    const u32x4 u = __builtin_bit_cast(u32x4, af[s]);
#pragma unroll
    for (int e = 0; e < 4; ++e) { s1 = dot2(u[e], 0x3f803f80u, s1); s2q = dot2(u[e], u[e], s2q); }
  }
  s1 = sum_halves(sum_rows(s1)); s2q = sum_halves(sum_rows(s2q));
  bar_all();

  if (lane < 16) *(LAS f32x2*)(smem + L_STAT + (kh * 64 + mt * 16 + lane) * 8) = f32x2{s1, s2q};
  KVRegs kv;
  const int k2o = (lane < 32 ? lane : 32) * 8, vo = (lane >> 5) * 352 + ((lane & 31) < 21 ? (lane & 31) : 21) * 16;
  auto load_tile = [&](int t) {
    kv.k1[t] = __builtin_bit_cast(bf16x8, ld16(rs, lane * 16, WS_KF1 + (w * 8 + t) * 1024));
    kv.k2[t] = __builtin_bit_cast(s16x4, ld8(rs, k2o, WS_KF2 + (w * 8 + t) * 272));
    kv.v[t][0] = __builtin_bit_cast(bf16x8, ld16(rs, vo, WS_VF + (w * 8 + t) * 1408));
    if (t < 7) kv.v[t][1] = __builtin_bit_cast(bf16x8, ld16(rs, vo + 704, WS_VF + (w * 8 + t) * 1408));
  };
  bar_lds();

  if (kh == 1) *(LAS u32x4*)(smem + L_W + 99 * 1024 + lane * 16) = late;
  {
    f32x4 acc[5] = {{0, 0, 0, 0}, {0, 0, 0, 0}, {0, 0, 0, 0}, {0, 0, 0, 0}, {0, 0, 0, 0}};
    f32x4 cs[5] = {{0, 0, 0, 0}, {0, 0, 0, 0}, {0, 0, 0, 0}, {0, 0, 0, 0}, {0, 0, 0, 0}};
    const bf16x8 ones = __builtin_bit_cast(bf16x8, u32x4{0x3f803f80u, 0x3f803f80u, 0x3f803f80u, 0x3f803f80u});
    gemm_half(af, smem + L_W + kh * 51200, lane, acc, [&](int t, const bf16x8 (&wf)[5]) {
      if ((t & 3) == mt) {
#pragma unroll
        for (int i = 0; i < 5; ++i) cs[i] = MFMA16(wf[i], ones, cs[i]);
      }
    });
    write_part(smem + L_PART, kh, mt, lane, acc);
    if ((lane & 15) == 0) {
#pragma unroll
      for (int i = 0; i < 5; ++i) *(LAS f32x4*)(smem + L_CS + w * 320 + i * 64 + (lane >> 4) * 16) = cs[i];
    }
  }
  if (w == 0) {
    u32x4 gv[4];
#pragma unroll
    for (int i = 0; i < 4; ++i) gv[i] = ld16(rs, lane * 128 + i * 8192, WS_FB);
#pragma unroll 1
      for (unsigned spin = 0; spin < SPIN_CAP; ++spin) {
        bool ok = true;
#pragma unroll
        for (int i = 0; i < 4; ++i) ok &= (gv[i][0] == tag) & (gv[i][2] == tag);
        if (__all(ok)) break;
        __builtin_amdgcn_s_sleep(POLL_SLEEP);
        CFENCE();
#pragma unroll
        for (int i = 0; i < 4; ++i)
          if ((gv[i][0] != tag) | (gv[i][2] != tag)) gv[i] = ld16(rs, lane * 128 + i * 8192, WS_FB);
      }
      CFENCE();
      float rm = __uint_as_float(gv[0][1]), kn = __uint_as_float(gv[0][3] & 0xffff0000u);
#pragma unroll
      for (int i = 1; i < 4; ++i) { rm = fmaxf(rm, __uint_as_float(gv[i][1])); kn = fmaxf(kn, __uint_as_float(gv[i][3] & 0xffff0000u)); }
      rm = max64(rm); kn = max64(kn);
      float neg = -4096.f;
#pragma unroll
      for (int i = 0; i < 4; ++i) neg = __uint_as_float(gv[i][1]) == rm ? fmaxf(neg, -(float)(gv[i][3] & 0xffffu)) : neg;
      neg = max64(neg);
      if (lane == 0) {
        *(LAS u32x4*)(smem + L_SUM) = u32x4{__float_as_uint(rm), (unsigned)(int)(-neg) & (S_ - 1), __float_as_uint(kn), 0u};
        if (bid == 0) st4(rs, tag, WS_EP);
      }
    }
  bar_lds();

  load_tile(0);
  kv.v[7][1] = __builtin_bit_cast(bf16x8, ld16(rs, vo + 704, WS_VF + (w * 8 + 7) * 1408));
  load_tile(1);
  SB();
  const u32x4 sumv = *(const LAS u32x4*)(smem + L_SUM);
  const float rmax = __uint_as_float(__builtin_amdgcn_readfirstlane(sumv[0]));
  const int ridx = __builtin_amdgcn_readfirstlane(sumv[1]);
  unsigned xs[5];
  float qn2 = 0.f;
  if (tid < 256) {
    const int b = tid >> 5, cc = tid & 31, tok = lane;
    const char LAS* pp = smem + L_PART + tok * 336 + w * 80;
    f32x4 cq = *(const LAS f32x4*)(smem + L_CS + (lane & 7) * 320 + w * 80 + (lane < 40 ? lane >> 3 : 0) * 16);
    const f32x2 st = *(const LAS f32x2*)(smem + L_STAT + tok * 8) + *(const LAS f32x2*)(smem + L_STAT + 512 + tok * 8);
#pragma unroll
    for (int e = 0; e < 4; ++e) { cq[e] += dpp_mov<0xB1>(cq[e]); cq[e] += dpp_mov<0x4E>(cq[e]); cq[e] += dpp_mov<0x141>(cq[e]); }
    const float mean = st[0] * (1.0f / D_);
    const float sd = ln_sd(fmaxf(fmaf(-mean, mean, st[1] * (1.0f / D_)), 0.f));
    const float mb = rbf(mean), sc = QSCALE / sd;
    if (w == 0) *(LAS unsigned*)(smem + L_STP + tok * 4) = pk2(mb, sd);
    unsigned qv[10];
#pragma unroll
    for (int j = 0; j < 5; ++j) {
      f32x4 a = *(const LAS f32x4*)(pp + j * 16) + *(const LAS f32x4*)(pp + 21504 + j * 16);
#pragma unroll
      for (int e = 0; e < 4; ++e) a[e] = fmaf(-mb, rdlane(cq[e], j * 8), a[e]) * sc;
      qv[2 * j] = pk2(a[0], a[1]); qv[2 * j + 1] = pk2(a[2], a[3]);
      qn2 = dot2(qv[2 * j], qv[2 * j], qn2); qn2 = dot2(qv[2 * j + 1], qv[2 * j + 1], qn2);
    }
    char LAS* qd = smem + L_Q + b * 1536;
    *(LAS u32x4*)(qd + cc * 16) = u32x4{qv[0], qv[1], qv[2], qv[3]};
    *(LAS u32x4*)(qd + (cc + 32) * 16) = u32x4{qv[4], qv[5], qv[6], qv[7]};
    *(LAS u32x2*)(qd + 1024 + cc * 8) = u32x2{qv[8], qv[9]};
    const float m = __builtin_amdgcn_sqrtf(qn2 * __uint_as_float(sumv[2])) * 1.01f;
    *(LAS u32x2*)(qd + 1024 + (cc + 32) * 8) = u32x2{pk2(-m, 0.f), 0u};
  } else {
#pragma unroll
    for (int i = 0; i < 12; ++i) *(LAS f32x4*)(smem + L_RING + i * 4096 + (tid - 256) * 16) = f32x4{0, 0, 0, 0};
  }
  load_tile(2);
  load_tile(3);
  bar_lds();

  const unsigned stp0 = *(const LAS unsigned*)(smem + L_STP + (tid / 10) * 4);
  const unsigned stp1 = *(const LAS unsigned*)(smem + L_STP + (tid < 128 ? (512 + tid) / 10 : 63) * 4);

  const f32x16 zero16 = {0, 0, 0, 0, 0, 0, 0, 0, 0, 0, 0, 0, 0, 0, 0, 0};
  f32x16 oa = zero16, ob = zero16;
  bf16x8 q1 = *(const LAS bf16x8*)(smem + L_Q + w * 1536 + lane * 16);
  s16x4 q2 = *(const LAS s16x4*)(smem + L_Q + w * 1536 + 1024 + lane * 8);
  f32x16 sa = MFMA32(kv.k1[0], q1, zero16), sb;
  sa = MFMA32K8(kv.k2[0], q2, sa);
  u32x4 p1 = {0, 0, 0, 0};
  att_block(kv, smem, lane, ((w + 7) & 7) + 8, ((w + 1) & 7) + 8, (w + 1) & 7, q1, q2, sa, sb, p1, ob, oa,
            [&](int i) {
              if (i < 4) load_tile(4 + i);
              if (i == 4) {
#pragma unroll
                for (int k = 0; k < 5; ++k)
                  asm volatile("global_load_dword %0, %1, %2 offset:%3" : "=v"(xs[k]) : "v"(lane * 4), "s"(x + (size_t)ridx * D_), "i"(k * 256) : "memory");
              }
            });
  asm volatile("s_waitcnt vmcnt(0)" : "+v"(xs[0]), "+v"(xs[1]), "+v"(xs[2]), "+v"(xs[3]), "+v"(xs[4]) :: "memory");
  if (w == 6) {
    float s = 0.f;
#pragma unroll
    for (int i = 0; i < 5; ++i) s = dot2(xs[i], 0x3f803f80u, s);
    const float mean = sum_halves(sum32(s)) * (1.0f / D_);
    f32x2 v2 = {0.f, 0.f};
#pragma unroll
    for (int i = 0; i < 5; ++i) { const f32x2 d = f32x2{bflo(xs[i]), bfhi(xs[i])} - mean; v2 += d * d; }
    const float sd = ln_sd(sum_halves(sum32(v2[0] + v2[1])) * (1.0f / D_));
    const float mb = rbf(mean), rc = 1.0f / sd;
#pragma unroll
    for (int i = 0; i < 5; ++i) *(LAS unsigned*)(smem + L_XS + i * 256 + lane * 4) = ln_pair(xs[i], mb, sd, rc);
  }
  DmaJob dj;
  if (w < 6) {
    const int ln = opaque(lane);
    const unsigned vo2 = w < 4 ? ln * (F_ * 2) : ln * 16;
#pragma unroll
    for (int i = 0; i < 5; ++i) dj.voff[i] = vo2;
    dj.sstr = w < 4 ? 64 * F_ * 2 : 1024;
    dj.sb = (w < 4 ? (const char*)((w < 2 ? wg : wu) + fcol) : (const char*)(wd + (size_t)fcol * D_)) + (size_t)(w & 1) * 5 * dj.sstr;
    dj.lds = lds0 + L_G + w * 5120;
    dj.on = has_ffn;
  } else {
    const int ln = opaque(lane);
    const char* qbase = (const char*)wo + hg * 160 + (size_t)(jj >> 3) * 204800 + (size_t)(w - 6) * 81920;
    touch4(qbase, (ln >> 1) * 1280 + (ln & 1) * 128, lds0 + L_FRED + 512);
    touch4(qbase + 40960, (ln >> 1) * 1280 + (ln & 1) * 128, lds0 + L_FRED + 512);
    if (w == 7) touch4(qbase + 81920, (ln >> 1) * 1280 + (ln & 1) * 128, lds0 + L_FRED + 512);
#pragma unroll
    for (int i = 0; i < 5; ++i) dj.voff[i] = 0;
    dj.sstr = 0; dj.sb = (const char*)wo; dj.lds = lds0; dj.on = 0;
  }
  u32x4 xe[2], ge[2];
  f32x4 yl[3];
  const bool sel_blk = (ridx >> 6) == qb;
#pragma unroll 1
  for (int attempt = 0;; ++attempt) {
#pragma unroll 1
    for (int st = 0; st < 8; st += 2) {
      int flag_off = -1;
      if (attempt == 0) {
        if (st == 4) {
          if (has_ffn && w < 4) {
            const int ln = opaque(lane);
            const int g = ln >> 4, q = (ln & 15) >> 2, pp = ln & 3;
            const char LAS* gb = smem + L_G + (pp >> 1) * 10240 + (8 * g + q) * 16 + (pp & 1) * 8 + w * 2560;
            const char LAS* xsp = smem + L_XS + w * 320 + g * 16;
            f32x4 a = {0, 0, 0, 0};
#pragma unroll
            for (int s = 0; s < 5; ++s)
              a = MFMA16(*(const LAS bf16x8*)(xsp + s * 64), cat8(trread(gb + s * 512), trread(gb + s * 512 + 64)), a);
            if (ln < 16) *(LAS float*)(smem + L_FRED + ln * 16 + w * 4) = a[0];
          }
        } else if (st == 6) {
          drain();
          if (has_ffn && w == 2) flag_off = WS_FY + pid * 128;
        }
      }
      if (attempt != 0 || st != 0)
        att_block(kv, smem, lane, ((w + st + 7) & 7) + 8, ((w + st + 1) & 7) + 8, (w + st + 1) & 7, q1, q2, sa, sb, p1, ob, oa,
                  [&](int i) { if (i == 5 && flag_off >= 0 && lane == 0) st4(rs, tag, flag_off); });
      unsigned waitmode = 0, yflag = 0;
      bool ylook = false;
      if (attempt == 0) {
        if (st == 2) {
          waitmode = 1;
          w_voff(opaque(lane), dj.voff);
          dj.sstr = 0;
          dj.sb = (const char*)wo + hg * 160 + (size_t)w * 40960; dj.lds = lds0 + L_W + w * 5120; dj.on = 1;
        } else if (st == 4) {
          dj.sb = (const char*)wo + hg * 160 + (size_t)(w < 2 ? 8 + w : 15 + w) * 40960;
          dj.lds = lds0 + (w < 2 ? L_W + (8 + w) * 5120 : L_WHI + (5 + w) * 5120); dj.on = w < 4;
          if (has_ffn && w >= 4) {
            const int ln = opaque(lane);
            const int cl = ln < 40 ? ln : 39;
            const f32x4 g4 = *(const LAS f32x4*)(smem + L_FRED + (ln & 15) * 16);
            u32x2 wv2[8];
#pragma unroll
            for (int c = 0; c < 8; ++c) wv2[c] = *(const LAS u32x2*)(smem + L_DN + c * 1280 + (w - 4) * 320 + cl * 8);
            const float gu = rbf((g4[0] + g4[1]) + (g4[2] + g4[3]));
            const float up = dpp_mov<0x128>(gu);
            const float hval = rbf(rbf(silu_f(gu)) * up);
            f32x4 y = {0, 0, 0, 0};
#pragma unroll
            for (int c = 0; c < 8; ++c) {
              const float hc = rdlane(hval, c);
              y[0] = fmaf(hc, bflo(wv2[c][0]), y[0]); y[1] = fmaf(hc, bfhi(wv2[c][0]), y[1]);
              y[2] = fmaf(hc, bflo(wv2[c][1]), y[2]); y[3] = fmaf(hc, bfhi(wv2[c][1]), y[3]);
            }
            if (ln < 40) st16(rs, __builtin_bit_cast(u32x4, y), ln * 16 + (ln >= 20 ? 2560 - 320 : 0),
                              WS_YP + (qb * 8 + (w - 4) * 2) * 2560 + hg * 320);
          }
        } else if (st == 6) {
          dj.sb = (const char*)wo + hg * 160 + (size_t)(w < 7 ? 10 + w : 19) * 40960;
          dj.lds = lds0 + L_WHI + (w < 7 ? w : 9) * 5120; dj.on = 1;
          if (has_ffn && w == 3) {
            asm volatile("global_load_dword %0, %1, %2 sc1" : "=v"(yflag) : "v"((lane & 7) * 128), "s"(ws + WS_FY + qb * 1024) : "memory");
            ylook = true;
          }
        }
      }
      att_block(kv, smem, lane, (w + st) & 7, (w + st + (st == 6 ? 1 : 2)) & 7, (w + st + 2) & 7, q1, q2, sa, sb, p1, oa, ob,
                [&](int i) {
                  dma_hook(dj, waitmode, i);
                  if (i == 3 && ylook) {
                    asm volatile("s_waitcnt vmcnt(0)" : "+v"(yflag) :: "memory");
                    if (!__all(yflag == tag)) poll_words(rs, (lane & 7) * 128, WS_FY + qb * 1024, tag);
                    CFENCE();
                    const int ln = opaque(lane);
#pragma unroll
                    for (int k = 0; k < 3; ++k)
                      yl[k] = __builtin_bit_cast(f32x4, ld16(rs, (ln < 60 && k * 60 + ln < 160) ? k * 960 + ln * 16 : 0x7ff00000, WS_YP + pid * 2560));
                  }
                });
      dj.on = 0;

    }
    __builtin_amdgcn_s_setprio(0);
    ob = MFMA32(kv.v[7][1], __builtin_bit_cast(bf16x8, p1), ob);
#pragma unroll
    for (int e = 0; e < 12; ++e) ob[e] += oa[e];

    const int tid4 = opaque(tid), ln4 = tid4 & 63;
    const float l = hi_half(ob[8]);
    const bool bad = attempt == 0 && __any(!(l >= 5.4e-20f && l <= 1e30f));
    LAS unsigned* ocnt = (LAS unsigned*)(smem + L_CNT);
    if (!bad) {
      const int b = (w + 7) & 7, hh = b >> 1, tok = qb * 64 + (b & 1) * 32 + (ln4 & 31), h4 = ln4 >> 5;
      const float inv = 1.0f / l;
      unsigned A0 = pk2(ob[0] * inv, ob[1] * inv), A1 = pk2(ob[2] * inv, ob[3] * inv);
      unsigned B0 = pk2(ob[4] * inv, ob[5] * inv), B1 = pk2(ob[6] * inv, ob[7] * inv);
      unsigned C0 = pk2(ob[8] * inv, ob[9] * inv), C1 = pk2(ob[10] * inv, ob[11] * inv);
      const int base = WS_O + ((tok >> 4) * 80 + hg * 10 + ((hh * 5) >> 1)) * 256 + (tok & 15) * 16;
      if ((hh & 1) == 0) {
        auto s0 = __builtin_amdgcn_permlane32_swap(A0, B0, false, false);
        auto s1 = __builtin_amdgcn_permlane32_swap(A1, B1, false, false);
        st16(rs, u32x4{s0[0], s1[0], s0[1], s1[1]}, base + h4 * 256);
        if (h4 == 0) st8(rs, u32x2{C0, C1}, base + 512);
      } else {
        auto s0 = __builtin_amdgcn_permlane32_swap(A0, C0, false, false);
        auto s1 = __builtin_amdgcn_permlane32_swap(A1, C1, false, false);
        st16(rs, h4 == 0 ? u32x4{s0[1], s1[1], B0, B1} : u32x4{B0, B1, s0[0], s1[0]}, base + 256 + h4 * 256);
        if (h4 == 0) st8(rs, u32x2{s0[0], s1[0]}, base + 8);
      }
    }
    const bool ysum = attempt == 0 && has_ffn && w == 3;
    if (ysum) {
      f32x4 a = (yl[0] + yl[1]) + yl[2];
#pragma unroll
      for (int e = 0; e < 4; ++e) a[e] = (a[e] + __shfl_down(a[e], 20)) + __shfl_down(a[e], 40);
      if (ln4 < 20) st16(rs, __builtin_bit_cast(u32x4, a), ln4 * 16, WS_YQ + (hg * 27 + qb) * 320);
    }
    drain();
    if (ln4 == 0 && lds_add_rtn(ocnt, bad ? 0x101u : 1u) == 7u && !bad) st4(rs, tag, WS_FC + pid * 128);
    if (ysum && ln4 == 0) st4(rs, tag, WS_FZ + hg * 4096 + qb * 128);

#pragma unroll
    for (int i = 0; i < 2; ++i) {
      int n = i * 512 + tid4; n = n < 640 ? n : 639;
      const int row = n / 10, ch = n - row * 10, tok = qb * 64 + row;
      xe[i] = *(const u32x4*)(x + (size_t)tok * D_ + hg * 80 + ch * 8);
    }
    if (w == 7) {
#pragma unroll 1
      for (unsigned spin = 0; spin < SPIN_CAP; ++spin) {
        CFENCE();
        const unsigned v = ld4(rs, (ln4 & 7) * 128, WS_FC + qb * 1024);
        if (__all(v == tag) || lds_get(ocnt) > 0xffu) break;
        __builtin_amdgcn_s_sleep(1);
      }
    }
    const bool selrow0 = qb * 64 + tid4 / 10 == ridx, selrow1 = qb * 64 + (512 + tid4) / 10 == ridx;
#pragma unroll
    for (int i = 0; i < 2; ++i) {
      const unsigned stp = i == 0 ? stp0 : stp1;
      const float mb = bflo(stp), sd = bfhi(stp), rc = 1.0f / sd;
#pragma unroll
      for (int e = 0; e < 4; ++e) ge[i][e] = ln_pair(xe[i][e], mb, sd, rc);
      if (!(i == 0 ? selrow0 : selrow1)) {
#pragma unroll
        for (int e = 0; e < 4; ++e) ge[i][e] = pk2(silu_f(bflo(ge[i][e])), silu_f(bfhi(ge[i][e])));
      }
    }
    bar_all();
    if (lds_get(ocnt) <= 0xffu) break;
    {
      const int c32 = ln4 & 31, h = ln4 >> 5;
#pragma unroll 1
      for (int b = 0; b < 8; ++b) {
        const bf16x8 fq1 = *(const LAS bf16x8*)(smem + L_Q + b * 1536 + ln4 * 16);
        s16x4 fq2 = *(const LAS s16x4*)(smem + L_Q + b * 1536 + 1024 + ln4 * 8);
        if (h == 1) fq2 = s16x4{0, 0, 0, 0};
        float mx = -INFINITY;
#pragma unroll
        for (int t = 0; t < 8; ++t) {
          f32x16 s = MFMA32(kv.k1[t], fq1, zero16);
          s = MFMA32K8(kv.k2[t], fq2, s);
#pragma unroll
          for (int e = 0; e < 16; ++e) mx = fmaxf(mx, s[e]);
        }
        mx = fmaxf(mx, __shfl_xor(mx, 32));
        if (h == 0) *(LAS float*)(smem + L_MAXS + (w * 256 + b * 32 + c32) * 4) = mx;
      }
      bar_lds();
      if (tid4 < 256) {
        float mx = -INFINITY;
#pragma unroll
        for (int i = 0; i < 8; ++i) mx = fmaxf(mx, *(LAS float*)(smem + L_MAXS + (i * 256 + tid4) * 4));
        const float hi = rbf(mx), lo = mx - hi;
        *(LAS u32x2*)(smem + L_Q + (tid4 >> 5) * 1536 + 1024 + ((tid4 & 31) + 32) * 8) = u32x2{pk2(-hi, -lo), 0u};
      }
      bar_lds();
#pragma unroll
      for (int i = 0; i < 6; ++i) *(LAS f32x4*)(smem + L_RING + i * 8192 + tid4 * 16) = f32x4{0, 0, 0, 0};
      bar_lds();
      q1 = *(const LAS bf16x8*)(smem + L_Q + w * 1536 + lane * 16);
      q2 = *(const LAS s16x4*)(smem + L_Q + w * 1536 + 1024 + lane * 8);
      sa = MFMA32(kv.k1[0], q1, zero16);
      sa = MFMA32K8(kv.k2[0], q2, sa);
      p1 = u32x4{0, 0, 0, 0};
      oa = zero16; ob = zero16;
    }
  }

  const int tid5 = opaque(tid), ln5 = tid5 & 63;
#pragma unroll
  for (int s = 0; s < 5; ++s)
    af[s] = __builtin_bit_cast(bf16x8, ld16(rs, ln5 * 16 + s * 1024, WS_O + ((qb * 4 + mt) * 20 + kh * 10) * 1024));
  LAS float* ys = (LAS float*)(smem + L_YS);
  {
    f32x4 acc[5] = {{0, 0, 0, 0}, {0, 0, 0, 0}, {0, 0, 0, 0}, {0, 0, 0, 0}, {0, 0, 0, 0}};
    gemm_half(af, smem + (kh ? L_WHI : L_W), ln5, acc, [&](int t, const bf16x8 (&)[5]) {
      if (t < 5) af[t + 5] = __builtin_bit_cast(bf16x8, ld16(rs, ln5 * 16 + (t + 5) * 1024, WS_O + ((qb * 4 + mt) * 20 + kh * 10) * 1024));
    });
    write_part(smem + L_PART2, kh, mt, ln5, acc);
  }

  f32x4 yt[9];
  if (sel_blk && w == 0) {
    poll_words(rs, (ln5 < 27 ? ln5 : 26) * 128, WS_FZ + hg * 4096, tag);
#pragma unroll
    for (int i = 0; i < 9; ++i) yt[i] = __builtin_bit_cast(f32x4, ld16(rs, (ln5 < 60 ? ln5 : 59) * 16 + i * 960, WS_YQ + hg * 8640));
  }
  bar_lds();
  auto finish = [&](int i, bool sel_pass) {
    const int n = i * 512 + tid5;
    const int row = n / 10, ch = n - row * 10, tok = qb * 64 + row;
    if (n < 640 && (tok == ridx) == sel_pass) {
      const char LAS* pp = smem + L_PART2 + row * 336 + ch * 32;
      const f32x4 s0 = *(const LAS f32x4*)(pp) + *(const LAS f32x4*)(pp + 21504);
      const f32x4 s1 = *(const LAS f32x4*)(pp + 16) + *(const LAS f32x4*)(pp + 21504 + 16);
      if (sel_pass) {
#pragma unroll
        for (int e = 0; e < 4; ++e)
          ge[i][e] = pk2(silu_f(rbf(bflo(ge[i][e]) + ys[ch * 8 + 2 * e])), silu_f(rbf(bfhi(ge[i][e]) + ys[ch * 8 + 2 * e + 1])));
      }
      u32x4 r;
      r[0] = pk2(bflo(xe[i][0]) + rbf(rbf(s0[0]) * bflo(ge[i][0])), bfhi(xe[i][0]) + rbf(rbf(s0[1]) * bfhi(ge[i][0])));
      r[1] = pk2(bflo(xe[i][1]) + rbf(rbf(s0[2]) * bflo(ge[i][1])), bfhi(xe[i][1]) + rbf(rbf(s0[3]) * bfhi(ge[i][1])));
      r[2] = pk2(bflo(xe[i][2]) + rbf(rbf(s1[0]) * bflo(ge[i][2])), bfhi(xe[i][2]) + rbf(rbf(s1[1]) * bfhi(ge[i][2])));
      r[3] = pk2(bflo(xe[i][3]) + rbf(rbf(s1[2]) * bflo(ge[i][3])), bfhi(xe[i][3]) + rbf(rbf(s1[3]) * bfhi(ge[i][3])));
      *(u32x4*)(out + (size_t)tok * D_ + hg * 80 + ch * 8) = r;
    }
  };
#pragma unroll 1
  for (int pass = 0; pass < 2; ++pass) {
    finish(0, pass == 1);
    finish(1, pass == 1);
    if (!sel_blk || pass == 1) break;
    if (w == 0) {
      f32x4 a = yt[0];
#pragma unroll
      for (int i = 1; i < 9; ++i) a += yt[i];
      const float rsig = rbf(sigm_f(rbf(rmax)));
#pragma unroll
      for (int e = 0; e < 4; ++e) a[e] = rbf(rbf((a[e] + __shfl_down(a[e], 20)) + __shfl_down(a[e], 40)) * rsig);
      if (ln5 < 20) *(LAS f32x4*)(ys + ln5 * 4) = a;
    }
    bar_lds();
  }

}

extern "C" void kernel_launch(void* const* d_in, const int* in_sizes, int n_in, void* d_out, int out_size,
                              void* d_ws, size_t ws_size, hipStream_t stream) {
  fused<<<256, 512, 0, stream>>>((const u16*)d_in[0], (const u16*)d_in[1], (const u16*)d_in[2], (const u16*)d_in[3],
                                 (const u16*)d_in[4], (const u16*)d_in[6], (const u16*)d_in[7], (const u16*)d_in[8],
                                 (const u16*)d_in[9], (char*)d_ws, (u16*)d_out);
}
```

```cpp
#include <hip/hip_runtime.h>
#include <hip/hip_bf16.h>
#include <stdint.h>

typedef __attribute__((ext_vector_type(8))) __bf16 bf16x8;
typedef __attribute__((ext_vector_type(2))) __bf16 bf16x2;
typedef __attribute__((ext_vector_type(4))) short s16x4;
typedef __attribute__((ext_vector_type(2))) float f32x2;
typedef __attribute__((ext_vector_type(4))) float f32x4;
typedef __attribute__((ext_vector_type(16))) float f32x16;
typedef __attribute__((ext_vector_type(2))) unsigned u32x2;
typedef __attribute__((ext_vector_type(4))) unsigned u32x4;
typedef unsigned short u16;
#define LAS __attribute__((address_space(3)))
#define DI __device__ __forceinline__
#define SB() __builtin_amdgcn_sched_barrier(0)
#define CFENCE() asm volatile("" ::: "memory")

constexpr int S_ = 2048, D_ = 640, F_ = 1728;

constexpr int WS_O = 0;
constexpr int WS_KF1 = WS_O + S_ * D_ * 2;
constexpr int WS_KF2 = WS_KF1 + 65536;
constexpr int WS_VF = WS_KF2 + 64 * 272;
constexpr int WS_YP = WS_VF + 64 * 1408;
constexpr int WS_YQ = WS_YP + 216 * D_ * 4;
constexpr int WS_FC = WS_YQ + 216 * 320 + 128 * 4;
constexpr int WS_FB = WS_FC + 32768;
constexpr int WS_FY = WS_FB + 32768;
constexpr int WS_FZ = WS_FY + 32768;
constexpr int WS_EP = WS_FZ + 32768;
static_assert(WS_FC % 128 == 0, "flag lines");
constexpr int WS_BYTES = WS_EP + 256;

constexpr int L_W = 0;
constexpr int L_SCR = 101376;
constexpr int L_WK = 102400, L_WV = 128000, L_X = 153600;
constexpr int L_WHI = 112640;
constexpr int L_RING = 51200;
constexpr int L_Q = 100352;
constexpr int L_PART = 112640;
constexpr int L_G = 112640, L_U = 122880, L_DN = 133120;
constexpr int L_FRED = 143360;
constexpr int L_XS = 161280;
constexpr int L_SUM = 162560;
constexpr int L_STAT = 155648;
constexpr int L_CS = 156672;
constexpr int L_STP = 159232;
constexpr int L_MAXS = L_RING;
constexpr int L_PART2 = L_RING;
constexpr int L_XSUM = L_RING + 43008;
constexpr int L_YS = L_RING + 48768;
constexpr int L_CNT = L_Q + 1024 + 256 + 4;
constexpr int LDS_BYTES = 163840;

constexpr float QSCALE = 0.22360679774997896f * 1.4426950408889634f;
constexpr unsigned SPIN_CAP = 1u << 18;
#define POLL_SLEEP 4

DI float bflo(unsigned v) { return __uint_as_float(v << 16); }
DI float bfhi(unsigned v) { return __uint_as_float(v & 0xffff0000u); }
DI unsigned pk2(float a, float b) {
  f32x2 t = {a, b};
  return __builtin_bit_cast(unsigned, __builtin_convertvector(t, bf16x2));
}
DI float rbf(float a) { return bflo(pk2(a, a)); }
DI float dot2(unsigned a, unsigned b, float c) {
  return __builtin_amdgcn_fdot2_f32_bf16(__builtin_bit_cast(bf16x2, a), __builtin_bit_cast(bf16x2, b), c, false);
}
DI s16x4 trread(const char LAS* p) {
  return __builtin_bit_cast(s16x4, __builtin_amdgcn_ds_read_tr16_b64_v4i16((LAS s16x4*)p));
}
DI bf16x8 cat8(s16x4 a, s16x4 b) {
  return __builtin_bit_cast(bf16x8, __builtin_shufflevector(a, b, 0, 1, 2, 3, 4, 5, 6, 7));
}
#define MFMA16(a, b, c) __builtin_amdgcn_mfma_f32_16x16x32_bf16((a), (b), (c), 0, 0, 0)
#define MFMA32(a, b, c) __builtin_amdgcn_mfma_f32_32x32x16_bf16((a), (b), (c), 0, 0, 0)
#define MFMA32K8(a, b, c) __builtin_amdgcn_mfma_f32_32x32x8bf16_1k((a), (b), (c), 0, 0, 0)
#define EXP2(x) __builtin_amdgcn_exp2f(x)
DI float sigm_f(float v) { return __builtin_amdgcn_rcpf(1.0f + EXP2(v * -1.4426950408889634f)); }
DI float silu_f(float v) { return v * sigm_f(v); }
DI void bar_lds() { asm volatile("s_waitcnt lgkmcnt(0)\n\ts_barrier" ::: "memory"); }
DI void bar_all() { asm volatile("s_waitcnt vmcnt(0) lgkmcnt(0)\n\ts_barrier" ::: "memory"); }
DI void drain() { asm volatile("s_waitcnt vmcnt(0)" ::: "memory"); }
DI void lds_add(LAS unsigned* p, unsigned v) {
  asm volatile("s_waitcnt lgkmcnt(0)" ::: "memory");
  __hip_atomic_fetch_add(p, v, __ATOMIC_RELAXED, __HIP_MEMORY_SCOPE_WORKGROUP);
}
DI unsigned lds_add_rtn(LAS unsigned* p, unsigned v) {
  asm volatile("s_waitcnt lgkmcnt(0)" ::: "memory");
  const unsigned r = __hip_atomic_fetch_add(p, v, __ATOMIC_RELAXED, __HIP_MEMORY_SCOPE_WORKGROUP);
  CFENCE();
  return r;
}
DI unsigned lds_get(LAS unsigned* p) {
  CFENCE();
  const unsigned r = __builtin_amdgcn_readfirstlane(__hip_atomic_load(p, __ATOMIC_RELAXED, __HIP_MEMORY_SCOPE_WORKGROUP));
  CFENCE();
  return r;
}
DI void lds_wait(LAS unsigned* p, unsigned target) {
#pragma unroll 1
  while (lds_get(p) < target) __builtin_amdgcn_s_sleep(1);
}
DI void dma16(const void* sbase, unsigned voff, unsigned lds) {
  unsigned keep;
  asm volatile("s_mov_b32 %0, m0\n\ts_mov_b32 m0, %3\n\ts_nop 0\n\tglobal_load_lds_dwordx4 %1, %2\n\ts_mov_b32 m0, %0"
               : "=&s"(keep) : "v"(voff), "s"(sbase), "s"(lds) : "memory");
}
DI void dma16_if(unsigned on, const void* sbase, unsigned voff, unsigned lds) {
  unsigned keep;
  on = __builtin_amdgcn_readfirstlane(on);
  lds = __builtin_amdgcn_readfirstlane(lds);
  sbase = (const void*)(((unsigned long long)__builtin_amdgcn_readfirstlane((unsigned)((unsigned long long)sbase >> 32)) << 32) |
                        (unsigned)__builtin_amdgcn_readfirstlane((unsigned)(unsigned long long)sbase));
  asm volatile("s_cmp_eq_u32 %4, 0\n\ts_cbranch_scc1 1f\n\ts_mov_b32 %0, m0\n\ts_mov_b32 m0, %3\n\ts_nop 1\n\t"
               "global_load_lds_dwordx4 %1, %2\n\ts_mov_b32 m0, %0\n1:"
               : "=&s"(keep) : "v"(voff), "s"(sbase), "s"(lds), "s"(on) : "memory", "scc");
}
DI void touch4(const void* sbase, unsigned voff, unsigned lds) {
  unsigned keep;
  asm volatile("s_mov_b32 %0, m0\n\ts_mov_b32 m0, %3\n\ts_nop 1\n\tglobal_load_lds_dword %1, %2\n\ts_mov_b32 m0, %0"
               : "=&s"(keep) : "v"(voff), "s"(sbase), "s"(lds) : "memory");
}
typedef __amdgpu_buffer_rsrc_t rsrc_t;
DI u32x4 ld16(rsrc_t r, int v, int s = 0) { return __builtin_amdgcn_raw_buffer_load_b128(r, v, s, 16); }
DI u32x2 ld8(rsrc_t r, int v, int s = 0) { return __builtin_amdgcn_raw_buffer_load_b64(r, v, s, 16); }
DI unsigned ld4(rsrc_t r, int v, int s = 0) { return __builtin_amdgcn_raw_buffer_load_b32(r, v, s, 16); }
DI void st16(rsrc_t r, u32x4 d, int v, int s = 0) { __builtin_amdgcn_raw_buffer_store_b128(d, r, v, s, 16); }
DI void st8(rsrc_t r, u32x2 d, int v, int s = 0) { __builtin_amdgcn_raw_buffer_store_b64(d, r, v, s, 16); }
DI void st4(rsrc_t r, unsigned d, int v, int s = 0) { __builtin_amdgcn_raw_buffer_store_b32(d, r, v, s, 16); }

template <int CTRL> DI float dpp_mov(float v) {
  return __builtin_bit_cast(float, __builtin_amdgcn_update_dpp(0, __builtin_bit_cast(int, v), CTRL, 0xf, 0xf, true));
}
DI float sum16(float v) {
  v += dpp_mov<0xB1>(v); v += dpp_mov<0x4E>(v); v += dpp_mov<0x141>(v); v += dpp_mov<0x140>(v);
  return v;
}
DI float sum_rows(float v) {
  const unsigned b = __float_as_uint(v);
  auto r = __builtin_amdgcn_permlane16_swap(b, b, false, false);
  return __uint_as_float(r[0]) + __uint_as_float(r[1]);
}
DI float sum_halves(float v) {
  const unsigned b = __float_as_uint(v);
  auto r = __builtin_amdgcn_permlane32_swap(b, b, false, false);
  return __uint_as_float(r[0]) + __uint_as_float(r[1]);
}
DI float hi_half(float v) {
  const unsigned b = __float_as_uint(v);
  auto r = __builtin_amdgcn_permlane32_swap(b, b, false, false);
  return __uint_as_float(r[1]);
}
DI float max64(float v) {
  v = fmaxf(v, dpp_mov<0xB1>(v)); v = fmaxf(v, dpp_mov<0x4E>(v)); v = fmaxf(v, dpp_mov<0x141>(v)); v = fmaxf(v, dpp_mov<0x140>(v));
  auto r = __builtin_amdgcn_permlane16_swap(__float_as_uint(v), __float_as_uint(v), false, false);
  v = fmaxf(__uint_as_float(r[0]), __uint_as_float(r[1]));
  auto q = __builtin_amdgcn_permlane32_swap(__float_as_uint(v), __float_as_uint(v), false, false);
  return fmaxf(__uint_as_float(q[0]), __uint_as_float(q[1]));
}
DI int opaque(int v) { asm volatile("" : "+v"(v)); return v; }
DI float sum32(float v) { return sum_rows(sum16(v)); }
DI float rdlane(float v, int l) { return __builtin_bit_cast(float, __builtin_amdgcn_readlane(__builtin_bit_cast(int, v), l)); }

DI float ln_sd(float var) { return rbf(sqrtf(rbf(rbf(var) + 1e-5f))); }
DI unsigned ln_pair(unsigned xp, float mb, float sd, float rc) {
  const unsigned dp = pk2(bflo(xp) - mb, bfhi(xp) - mb);
  const f32x2 d = {bflo(dp), bfhi(dp)};
  const f32x2 q0 = d * rc;
  const f32x2 a = __builtin_elementwise_fma(__builtin_elementwise_fma(-q0, f32x2{sd, sd}, d), f32x2{rc, rc}, q0);
  return pk2(a[0], a[1]);
}
DI void load_piece(const u16* __restrict__ xrow0, int lane, u32x4 (&xp)[10]) {
#pragma unroll
  for (int i = 0; i < 5; ++i) {
    const int n = i * 64 + lane, row = n / 20, ch = n - row * 20;
    xp[i] = *(const u32x4*)((const char*)xrow0 + row * 1280 + ch * 16);
    xp[5 + i] = *(const u32x4*)((const char*)xrow0 + row * 1280 + ch * 16 + 320);
  }
}
DI void bounce_piece(char LAS* blk, int lane, const u32x4 (&xp)[10], bf16x8 (&af)[10]) {
#pragma unroll
  for (int hf = 0; hf < 2; ++hf) {
#pragma unroll
    for (int i = 0; i < 5; ++i) {
      const int n = i * 64 + lane, row = n / 20, ch = n - row * 20;
      *(LAS u32x4*)(blk + row * 336 + ch * 16) = xp[hf * 5 + i];
    }
#pragma unroll
    for (int s = 0; s < 5; ++s) af[hf * 5 + s] = *(const LAS bf16x8*)(blk + (lane & 15) * 336 + (lane >> 4) * 16 + s * 64);
  }
}
DI void w_voff(int lane, unsigned (&voff)[5]) {
#pragma unroll
  for (int i = 0; i < 5; ++i) {
    const int n = i * 64 + lane, p = n / 10, ch = n - p * 10;
    const int k = (p & ~12) | ((p & 4) << 1) | ((p & 8) >> 1);
    voff[i] = k * 1280 + ch * 16;
  }
}
template <class F>
DI void gemm_half(bf16x8 (&af)[10], const char LAS* wimg, int lane, f32x4 (&acc)[5], F side) {
  const int g = lane >> 4, q = (lane & 15) >> 2, pp = lane & 3;
  const char LAS* wb = wimg + (16 * (g >> 1) + 4 * (g & 1) + q) * 160 + 8 * pp;
  bf16x8 wf[3][5];
#pragma unroll
  for (int p = 0; p < 2; ++p)
#pragma unroll
    for (int i = 0; i < 5; ++i) wf[p][i] = cat8(trread(wb + p * 5120 + i * 32), trread(wb + p * 5120 + 1280 + i * 32));
#pragma unroll
  for (int s = 0; s < 10; ++s) {
#pragma unroll
    for (int i = 0; i < 5; ++i) {
      acc[i] = MFMA16(wf[s % 3][i], af[s], acc[i]);
      SB();
      if (s < 8) wf[(s + 2) % 3][i] = cat8(trread(wb + (s + 2) * 5120 + i * 32), trread(wb + (s + 2) * 5120 + 1280 + i * 32));
      SB();
    }
    side(s, wf[s % 3]);
    SB();
  }
}
DI void write_part(char LAS* part, int kh, int mt, int lane, const f32x4 (&acc)[5]) {
  char LAS* p = part + kh * 21504 + (mt * 16 + (lane & 15)) * 336 + (lane >> 4) * 16;
#pragma unroll
  for (int i = 0; i < 5; ++i) *(LAS f32x4*)(p + i * 64) = acc[i];
}

struct KVRegs { bf16x8 k1[8]; s16x4 k2[8]; bf16x8 v[8][2]; };
struct DmaJob { const char* sb; unsigned sstr, lds, on; unsigned voff[5]; };

#define EXP_PK(dst, S, b) { dst[0] = pk2(EXP2(S[b]), EXP2(S[b + 1])); dst[1] = pk2(EXP2(S[b + 2]), EXP2(S[b + 3])); }
template <bool NEWBLK, int PRIO>
DI void att_step(f32x16& sc, f32x16& sn, const bf16x8& k1n, const s16x4& k2n, const bf16x8& q1n, const s16x4& q2n,
                 const bf16x8& v0, const bf16x8& v1prev, u32x4& p1, f32x16& oprev, f32x16& ocur) {
  const f32x16 zero16 = {0, 0, 0, 0, 0, 0, 0, 0, 0, 0, 0, 0, 0, 0, 0, 0};
  u32x2 a, b, c, d;
  if (PRIO >= 0) __builtin_amdgcn_s_setprio(PRIO);
  sn = MFMA32(k1n, q1n, zero16); SB();
  EXP_PK(a, sc, 0); SB();
  sn = MFMA32K8(k2n, q2n, sn); SB();
  EXP_PK(b, sc, 4); SB();
  oprev = MFMA32(v1prev, __builtin_bit_cast(bf16x8, p1), oprev); SB();
  EXP_PK(c, sc, 8); SB();
  const u32x4 p0 = {a[0], a[1], b[0], b[1]};
  if (NEWBLK) ocur = MFMA32(v0, __builtin_bit_cast(bf16x8, p0), ocur);
  else ocur = MFMA32(v0, __builtin_bit_cast(bf16x8, p0), oprev);
  SB();
  EXP_PK(d, sc, 12); SB();
  p1 = u32x4{c[0], c[1], d[0], d[1]};
}
template <class H>
DI void att_block(const KVRegs& kv, char LAS* smem, int lane, int sout, int sin, int bnext, bf16x8& q1, s16x4& q2,
                  f32x16& sa, f32x16& sb, u32x4& p1, f32x16& oprev, f32x16& ocur, H hook) {
  LAS f32x4* so = (LAS f32x4*)(smem + L_RING + sout * 3072) + lane;
  LAS f32x4* si = (LAS f32x4*)(smem + L_RING + sin * 3072) + lane;
  att_step<true, -1>(sa, sb, kv.k1[1], kv.k2[1], q1, q2, kv.v[0][0], kv.v[7][1], p1, oprev, ocur);
  hook(0); SB();
  att_step<false, 1>(sb, sa, kv.k1[2], kv.k2[2], q1, q2, kv.v[1][0], kv.v[0][1], p1, ocur, ocur);
  so[0] = f32x4{oprev[0], oprev[1], oprev[2], oprev[3]};
  hook(1); SB();
  att_step<false, -1>(sa, sb, kv.k1[3], kv.k2[3], q1, q2, kv.v[2][0], kv.v[1][1], p1, ocur, ocur);
  so[64] = f32x4{oprev[4], oprev[5], oprev[6], oprev[7]};
  hook(2); SB();
  att_step<false, 0>(sb, sa, kv.k1[4], kv.k2[4], q1, q2, kv.v[3][0], kv.v[2][1], p1, ocur, ocur);
  so[128] = f32x4{oprev[8], oprev[9], oprev[10], oprev[11]};
  hook(3); SB();
  att_step<false, -1>(sa, sb, kv.k1[5], kv.k2[5], q1, q2, kv.v[4][0], kv.v[3][1], p1, ocur, ocur);
  hook(4);
  bar_lds();
  const bf16x8 q1n = *(const LAS bf16x8*)(smem + L_Q + bnext * 1536 + lane * 16);
  const s16x4 q2n = *(const LAS s16x4*)(smem + L_Q + bnext * 1536 + 1024 + lane * 8);
  SB();
  att_step<false, 3>(sb, sa, kv.k1[6], kv.k2[6], q1, q2, kv.v[5][0], kv.v[4][1], p1, ocur, ocur);
  {
    const f32x4 i0 = si[0], i1 = si[64], i2 = si[128];
#pragma unroll
    for (int e = 0; e < 4; ++e) { oprev[e] = i0[e]; oprev[4 + e] = i1[e]; oprev[8 + e] = i2[e]; }
  }
  hook(5); SB();
  att_step<false, -1>(sa, sb, kv.k1[7], kv.k2[7], q1, q2, kv.v[6][0], kv.v[5][1], p1, ocur, ocur);
  hook(6); SB();
  att_step<false, 2>(sb, sa, kv.k1[0], kv.k2[0], q1n, q2n, kv.v[7][0], kv.v[6][1], p1, ocur, ocur);
  q1 = q1n; q2 = q2n;
}
DI void dma_hook(const DmaJob& dj, unsigned waitmode, int i) {
  if (i == 4) {
    asm volatile("s_cmp_eq_u32 %0, 0\n\ts_cbranch_scc1 1f\n\ts_waitcnt vmcnt(3)\n\ts_cmp_eq_u32 %0, 1\n\ts_cbranch_scc1 1f\n\t"
                 "s_waitcnt vmcnt(0)\n1:" :: "s"(__builtin_amdgcn_readfirstlane(waitmode)) : "memory", "scc");
  } else if (i != 1) {
    const int c = i == 0 ? 0 : (i < 4 ? i - 1 : i - 2);
    dma16_if(dj.on, dj.sb + (size_t)c * dj.sstr, dj.voff[c], dj.lds + c * 1024);
  }
}
DI void poll_words(rsrc_t rs, int voff, int soff, unsigned tag) {
#pragma unroll 1
  for (unsigned spin = 0; spin < SPIN_CAP; ++spin) {
    CFENCE();
    const unsigned v = ld4(rs, voff, soff);
    if (__all(v == tag)) break;
    __builtin_amdgcn_s_sleep(1);
  }
  CFENCE();
}

__global__ __launch_bounds__(512, 2) void fused(const u16* __restrict__ x, const u16* __restrict__ wq,
                                                const u16* __restrict__ wk, const u16* __restrict__ wv,
                                                const u16* __restrict__ wo, const u16* __restrict__ wr,
                                                const u16* __restrict__ wg, const u16* __restrict__ wu,
                                                const u16* __restrict__ wd, char* ws, u16* __restrict__ out) {
  __shared__ __attribute__((aligned(16))) char smem_[LDS_BYTES];
  char LAS* smem = (char LAS*)smem_;
  const unsigned lds0 = (unsigned)(size_t)smem;
  const int tid = threadIdx.x, lane = tid & 63;
  const int w = __builtin_amdgcn_readfirstlane(tid >> 6);
  {
    const char* pc = (const char*)__builtin_amdgcn_s_getpc();
    touch4(pc + w * 4352, (lane < 34 ? lane : 33) * 128, lds0 + L_SCR + 512);
  }
  const int bid = blockIdx.x, xcd = bid & 7, jj = bid >> 3;
  const int qb = xcd * 4 + (jj >> 3), hg = jj & 7, pid = qb * 8 + hg;
  const bool has_ffn = qb < 27;
  const int fcol = pid * 8;
  const int tok0 = pid * 8, tile = tok0 >> 5;
  const rsrc_t rs = __builtin_amdgcn_make_buffer_rsrc(ws, 0, WS_BYTES, 0x00020000);
  LAS float* scr = (LAS float*)(smem + L_SCR);
  LAS unsigned* cnt = (LAS unsigned*)(smem + L_SCR + 96);

  unsigned tag = 0;
  u32x4 late = {0, 0, 0, 0};
  const int mt = w & 3, kh = w >> 2;
  bf16x8 af[10];
  const u16* xpiece = x + (size_t)(qb * 64 + mt * 16) * D_ + kh * 320;
  if (w < 4) {
    const int half = lane >> 5, l32 = lane & 31, tl = 2 * w + half;
    u32x2 xv[5], wv5[5];
    u32x4 xp[10];
#pragma unroll
    for (int i = 0; i < 5; ++i) xv[i] = *(const u32x2*)(x + (size_t)(tok0 + tl) * D_ + i * 128 + l32 * 4);
    const unsigned ep = ld4(rs, WS_EP);
#pragma unroll
    for (int i = 0; i < 5; ++i) wv5[i] = *(const u32x2*)(wr + i * 128 + l32 * 4);
    if (tid < 2) *((LAS u32x4*)cnt + tid) = u32x4{0, 0, 0, 0};
    bar_lds();
    load_piece(xpiece, lane, xp);
    SB();
    float s = 0.f;
#pragma unroll
    for (int i = 0; i < 5; ++i) { s = dot2(xv[i][0], 0x3f803f80u, s); s = dot2(xv[i][1], 0x3f803f80u, s); }

    const float mean = sum32(s) * (1.0f / D_);
    f32x2 v2 = {0.f, 0.f};
#pragma unroll
    for (int i = 0; i < 10; ++i) {
      const f32x2 d = f32x2{bflo(xv[i >> 1][i & 1]), bfhi(xv[i >> 1][i & 1])} - mean;
      v2 += d * d;
    }
    const float sd = ln_sd(sum32(v2[0] + v2[1]) * (1.0f / D_));
    const float mb = rbf(mean), rc = 1.0f / sd;
#pragma unroll
    for (int i = 0; i < 5; ++i) {
      xv[i] = u32x2{ln_pair(xv[i][0], mb, sd, rc), ln_pair(xv[i][1], mb, sd, rc)};
      *(LAS u32x2*)(smem + L_X + tl * 1280 + (((i * 16 + (l32 >> 1)) ^ tl) * 16) + (l32 & 1) * 8) = xv[i];
    }
    if (lane == 0) lds_add(cnt + 0, 1);
    float racc = 0.f;
#pragma unroll
    for (int i = 0; i < 5; ++i) { racc = dot2(xv[i][0], wv5[i][0], racc); racc = dot2(xv[i][1], wv5[i][1], racc); }
    racc = sum32(racc);
    if (l32 == 0) scr[tl] = racc;
    tag = __builtin_amdgcn_readfirstlane(ep) + 1u;
    lds_wait(cnt + 0, 6);

    const int g = lane >> 4, c = lane & 15, q = c >> 2, pp = lane & 3, r8 = c & 7;
    const int mat = w >> 1, nt = w & 1;
    const char LAS* wb = smem + L_WK + mat * 25600 + (8 * g + q) * 40 + nt * 32 + 8 * pp;
    const char LAS* xb0 = smem + L_X + r8 * 1280 + (g ^ r8) * 16;
    const char LAS* xb1 = smem + L_X + r8 * 1280 + ((4 + g) ^ r8) * 16;
    bf16x8 wf[20], xf[20];
#pragma unroll
    for (int s2 = 0; s2 < 20; ++s2) {
      xf[s2] = *(const LAS bf16x8*)(((s2 & 1) ? xb1 : xb0) + (s2 >> 1) * 128);
      wf[s2] = cat8(trread(wb + s2 * 1280), trread(wb + s2 * 1280 + 160));
    }
    f32x4 a0 = {0, 0, 0, 0}, a1 = {0, 0, 0, 0};
    if (mat == 0) {
#pragma unroll
      for (int s2 = 0; s2 < 20; s2 += 2) { a0 = MFMA16(wf[s2], xf[s2], a0); a1 = MFMA16(wf[s2 + 1], xf[s2 + 1], a1); }
    } else {
#pragma unroll
      for (int s2 = 0; s2 < 20; s2 += 2) { a0 = MFMA16(xf[s2], wf[s2], a0); a1 = MFMA16(xf[s2 + 1], wf[s2 + 1], a1); }
    }
    if (lane == 0) lds_add(cnt + 2, 1);
    a0 += a1;
    u32x2 pk = {pk2(a0[0], a0[1]), pk2(a0[2], a0[3])};
    int soff; bool son;
    if (mat == 0) {
      float n2 = dot2(pk[0], pk[0], dot2(pk[1], pk[1], 0.f));
      const int r = (tok0 & 31) + c;
      if (nt == 0) {
        soff = WS_KF1 + tile * 1024 + (r + 32 * (g >> 1)) * 16 + (g & 1) * 8; son = c < 8;
        n2 = sum_halves(sum_rows(n2));
      } else {
        if (lane == 8) pk = u32x2{0x3f803f80u, 0u};
        soff = WS_KF2 + tile * 272 + (lane < 8 ? r : 32) * 8; son = lane <= 8;
      }
      if (lane < 8) scr[8 + nt * 8 + lane] = n2;
    } else {
      const int d = 16 * nt + c, u = (tok0 & 31) >> 3;
      if (d == 20) pk = u32x2{0x3f803f80u, 0x3f803f80u};
      if (d == 21) pk = u32x2{0u, 0u};
      soff = WS_VF + ((tile * 2 + (u >> 1)) * 2 + g) * 352 + d * 16 + (u & 1) * 8; son = g < 2 && d < 22;
    }
    if (son) st8(rs, pk, soff);
    lds_wait(cnt + 2, 4);
    bounce_piece(smem + L_WK + w * 5376, lane, xp, af);
    drain();
    if (lane == 0 && lds_add_rtn(cnt + 3, 1) == 3) {
      f32x4 sv[6];
#pragma unroll
      for (int i = 0; i < 6; ++i) sv[i] = *(const LAS f32x4*)(scr + 4 * i);
      float rm = sv[0][0], km = sv[2][0] + sv[4][0]; int ri = 0;
#pragma unroll
      for (int i = 1; i < 8; ++i) {
        const float rr = sv[i >> 2][i & 3];
        const bool t = rr > rm;
        rm = t ? rr : rm; ri = t ? i : ri;
        km = fmaxf(km, sv[2 + (i >> 2)][i & 3] + sv[4 + (i >> 2)][i & 3]);
      }
      const unsigned kb = (__float_as_uint(km) + 0xffffu) & 0xffff0000u;
      st16(rs, u32x4{tag, __float_as_uint(rm), tag, kb | (unsigned)(tok0 + ri)}, WS_FB + pid * 128);
    }

    lds_wait(cnt + 4, 4);
    {
      unsigned voff[5];
      w_voff(lane, voff);
#pragma unroll
      for (int c = 0; c < 5; ++c)
        dma16((const char*)wq + hg * 160 + (size_t)w * 40960, voff[c], lds0 + L_W + w * 5120 + c * 1024);
    }
  } else {
    u32x4 xp[10];
    unsigned voff[5];
    if (w < 6) {
      u32x4 a[25];
      const char* src = (const char*)(w == 4 ? wk : wv) + lane * 16;
      const int rot = ((jj + xcd * 3) % 5) * 5;
#pragma unroll
      for (int i = 0; i < 25; ++i) {
        a[i] = *(const u32x4*)(src + (i + rot >= 25 ? i + rot - 25 : i + rot) * 1024);
        if (i == 0) bar_lds();
      }
      w_voff(lane, voff);
      SB();
      char LAS* dst = smem + (w == 4 ? L_WK : L_WV) + lane * 16;
#pragma unroll
      for (int i = 0; i < 25; ++i) *(LAS u32x4*)(dst + (i + rot >= 25 ? i + rot - 25 : i + rot) * 1024) = a[i];
      if (lane == 0) lds_add(cnt + 0, 1);
    } else {
      bar_lds();
      w_voff(lane, voff);
      int G = ((jj >> 3) * 4 + (xcd >> 1) + (w - 6) * 8) % 15;
#pragma unroll 1
      for (int i = w - 6; i < 8; ++i) {
        const char* sb = (const char*)wq + hg * 160 + (size_t)(5 + G) * 40960;
#pragma unroll
        for (int c = 0; c < 5; ++c) dma16_if(G * 5 + c != 74, sb, voff[c], lds0 + L_W + (5 + G) * 5120 + c * 1024);
        G = G == 14 ? 0 : G + 1;
      }
    }

    load_piece(xpiece, lane, xp);
    late = *(const u32x4*)((const char*)wq + hg * 160 + 19 * 40960 + voff[4]);
    bounce_piece(smem + L_W + (w - 4) * 5376, lane, xp, af);
#pragma unroll
    for (int s = 0; s < 10; ++s) asm volatile("" : "+v"(af[s]));
    asm volatile("" : "+v"(late));
    if (lane == 0) lds_add(cnt + 4, 1);

    if (w == 4) {
      lds_wait(cnt + 4, 4);
#pragma unroll
      for (int c = 0; c < 5; ++c) dma16((const char*)wq + hg * 160 + 4 * 40960, voff[c], lds0 + L_W + 4 * 5120 + c * 1024);
    }

  }
  float s1 = 0.f, s2q = 0.f;
#pragma unroll
  for (int s = 0; s < 10; ++s) {
    const u32x4 u = __builtin_bit_cast(u32x4, af[s]);
#pragma unroll
    for (int e = 0; e < 4; ++e) { s1 = dot2(u[e], 0x3f803f80u, s1); s2q = dot2(u[e], u[e], s2q); }
  }
  s1 = sum_halves(sum_rows(s1)); s2q = sum_halves(sum_rows(s2q));
  bar_all();

  if (lane < 16) *(LAS f32x2*)(smem + L_STAT + (kh * 64 + mt * 16 + lane) * 8) = f32x2{s1, s2q};
  KVRegs kv;
  const int k2o = (lane < 32 ? lane : 32) * 8, vo = (lane >> 5) * 352 + ((lane & 31) < 21 ? (lane & 31) : 21) * 16;
  auto load_tile = [&](int t) {
    kv.k1[t] = __builtin_bit_cast(bf16x8, ld16(rs, lane * 16, WS_KF1 + (w * 8 + t) * 1024));
    kv.k2[t] = __builtin_bit_cast(s16x4, ld8(rs, k2o, WS_KF2 + (w * 8 + t) * 272));
    kv.v[t][0] = __builtin_bit_cast(bf16x8, ld16(rs, vo, WS_VF + (w * 8 + t) * 1408));
    if (t < 7) kv.v[t][1] = __builtin_bit_cast(bf16x8, ld16(rs, vo + 704, WS_VF + (w * 8 + t) * 1408));
  };
  u32x4 gv[4];
  if (w == 0) {
#pragma unroll
    for (int i = 0; i < 4; ++i)
      asm volatile("global_load_dwordx4 %0, %1, %2 sc1" : "=v"(gv[i]) : "v"(lane * 128 + i * 8192), "s"(ws + WS_FB) : "memory");
  }
  bar_lds();

  if (kh == 1) *(LAS u32x4*)(smem + L_W + 99 * 1024 + lane * 16) = late;
  {
    f32x4 acc[5] = {{0, 0, 0, 0}, {0, 0, 0, 0}, {0, 0, 0, 0}, {0, 0, 0, 0}, {0, 0, 0, 0}};
    f32x4 cs[5] = {{0, 0, 0, 0}, {0, 0, 0, 0}, {0, 0, 0, 0}, {0, 0, 0, 0}, {0, 0, 0, 0}};
    const bf16x8 ones = __builtin_bit_cast(bf16x8, u32x4{0x3f803f80u, 0x3f803f80u, 0x3f803f80u, 0x3f803f80u});
    gemm_half(af, smem + L_W + kh * 51200, lane, acc, [&](int t, const bf16x8 (&wf)[5]) {
      if ((t & 3) == mt) {
#pragma unroll
        for (int i = 0; i < 5; ++i) cs[i] = MFMA16(wf[i], ones, cs[i]);
      }
    });
    write_part(smem + L_PART, kh, mt, lane, acc);
    if ((lane & 15) == 0) {
#pragma unroll
      for (int i = 0; i < 5; ++i) *(LAS f32x4*)(smem + L_CS + w * 320 + i * 64 + (lane >> 4) * 16) = cs[i];
    }
  }
  if (w == 0) {
    asm volatile("s_waitcnt vmcnt(0)" : "+v"(gv[0]), "+v"(gv[1]), "+v"(gv[2]), "+v"(gv[3]) :: "memory");
#pragma unroll 1
      for (unsigned spin = 0; spin < SPIN_CAP; ++spin) {
        bool ok = true;
#pragma unroll
        for (int i = 0; i < 4; ++i) ok &= (gv[i][0] == tag) & (gv[i][2] == tag);
        if (__all(ok)) break;
        if (spin) __builtin_amdgcn_s_sleep(POLL_SLEEP);
        CFENCE();
#pragma unroll
        for (int i = 0; i < 4; ++i)
          if ((gv[i][0] != tag) | (gv[i][2] != tag)) gv[i] = ld16(rs, lane * 128 + i * 8192, WS_FB);
      }
      CFENCE();
      float rm = __uint_as_float(gv[0][1]), kn = __uint_as_float(gv[0][3] & 0xffff0000u);
#pragma unroll
      for (int i = 1; i < 4; ++i) { rm = fmaxf(rm, __uint_as_float(gv[i][1])); kn = fmaxf(kn, __uint_as_float(gv[i][3] & 0xffff0000u)); }
      rm = max64(rm); kn = max64(kn);
      float neg = -4096.f;
#pragma unroll
      for (int i = 0; i < 4; ++i) neg = __uint_as_float(gv[i][1]) == rm ? fmaxf(neg, -(float)(gv[i][3] & 0xffffu)) : neg;
      neg = max64(neg);
      if (lane == 0) {
        *(LAS u32x4*)(smem + L_SUM) = u32x4{__float_as_uint(rm), (unsigned)(int)(-neg) & (S_ - 1), __float_as_uint(kn), tag};
        if (bid == 0) st4(rs, tag, WS_EP);
      }
    }
  bar_lds();

  load_tile(0);
  kv.v[7][1] = __builtin_bit_cast(bf16x8, ld16(rs, vo + 704, WS_VF + (w * 8 + 7) * 1408));
  load_tile(1);
  SB();
  const u32x4 sumv = *(const LAS u32x4*)(smem + L_SUM);
  const float rmax = __uint_as_float(__builtin_amdgcn_readfirstlane(sumv[0]));
  const int ridx = __builtin_amdgcn_readfirstlane(sumv[1]);
  tag = __builtin_amdgcn_readfirstlane(sumv[3]);
  unsigned xs[5];
  float qn2 = 0.f;
  if (tid < 256) {
    const int b = tid >> 5, cc = tid & 31, tok = lane;
    const char LAS* pp = smem + L_PART + tok * 336 + w * 80;
    f32x4 cq = *(const LAS f32x4*)(smem + L_CS + (lane & 7) * 320 + w * 80 + (lane < 40 ? lane >> 3 : 0) * 16);
    const f32x2 st = *(const LAS f32x2*)(smem + L_STAT + tok * 8) + *(const LAS f32x2*)(smem + L_STAT + 512 + tok * 8);
#pragma unroll
    for (int e = 0; e < 4; ++e) { cq[e] += dpp_mov<0xB1>(cq[e]); cq[e] += dpp_mov<0x4E>(cq[e]); cq[e] += dpp_mov<0x141>(cq[e]); }
    const float mean = st[0] * (1.0f / D_);
    const float sd = ln_sd(fmaxf(fmaf(-mean, mean, st[1] * (1.0f / D_)), 0.f));
    const float mb = rbf(mean), sc = QSCALE / sd;
    if (w == 0) *(LAS unsigned*)(smem + L_STP + tok * 4) = pk2(mb, sd);
    unsigned qv[10];
#pragma unroll
    for (int j = 0; j < 5; ++j) {
      f32x4 a = *(const LAS f32x4*)(pp + j * 16) + *(const LAS f32x4*)(pp + 21504 + j * 16);
#pragma unroll
      for (int e = 0; e < 4; ++e) a[e] = fmaf(-mb, rdlane(cq[e], j * 8), a[e]) * sc;
      qv[2 * j] = pk2(a[0], a[1]); qv[2 * j + 1] = pk2(a[2], a[3]);
      qn2 = dot2(qv[2 * j], qv[2 * j], qn2); qn2 = dot2(qv[2 * j + 1], qv[2 * j + 1], qn2);
    }
    char LAS* qd = smem + L_Q + b * 1536;
    *(LAS u32x4*)(qd + cc * 16) = u32x4{qv[0], qv[1], qv[2], qv[3]};
    *(LAS u32x4*)(qd + (cc + 32) * 16) = u32x4{qv[4], qv[5], qv[6], qv[7]};
    *(LAS u32x2*)(qd + 1024 + cc * 8) = u32x2{qv[8], qv[9]};
    const float m = __builtin_amdgcn_sqrtf(qn2 * __uint_as_float(sumv[2])) * 1.01f;
    *(LAS u32x2*)(qd + 1024 + (cc + 32) * 8) = u32x2{pk2(-m, 0.f), 0u};
  } else {
#pragma unroll
    for (int i = 0; i < 12; ++i) *(LAS f32x4*)(smem + L_RING + i * 4096 + (tid - 256) * 16) = f32x4{0, 0, 0, 0};
  }
  bar_lds();

  load_tile(2);
  load_tile(3);
  SB();
  const unsigned stp0 = *(const LAS unsigned*)(smem + L_STP + (tid / 10) * 4);
  const unsigned stp1 = *(const LAS unsigned*)(smem + L_STP + (tid < 128 ? (512 + tid) / 10 : 63) * 4);

  const f32x16 zero16 = {0, 0, 0, 0, 0, 0, 0, 0, 0, 0, 0, 0, 0, 0, 0, 0};
  f32x16 oa = zero16, ob = zero16;
  bf16x8 q1 = *(const LAS bf16x8*)(smem + L_Q + w * 1536 + lane * 16);
  s16x4 q2 = *(const LAS s16x4*)(smem + L_Q + w * 1536 + 1024 + lane * 8);
  f32x16 sa = MFMA32(kv.k1[0], q1, zero16), sb;
  sa = MFMA32K8(kv.k2[0], q2, sa);
  u32x4 p1 = {0, 0, 0, 0};
  att_block(kv, smem, lane, ((w + 7) & 7) + 8, ((w + 1) & 7) + 8, (w + 1) & 7, q1, q2, sa, sb, p1, ob, oa,
            [&](int i) {
              if (i < 4) load_tile(4 + i);
              if (i == 4) {
#pragma unroll
                for (int k = 0; k < 5; ++k)
                  asm volatile("global_load_dword %0, %1, %2 offset:%3" : "=v"(xs[k]) : "v"(lane * 4), "s"(x + (size_t)ridx * D_), "i"(k * 256) : "memory");
              }
            });
  asm volatile("s_waitcnt vmcnt(0)" : "+v"(xs[0]), "+v"(xs[1]), "+v"(xs[2]), "+v"(xs[3]), "+v"(xs[4]) :: "memory");
  if (w == 6) {
    float s = 0.f;
#pragma unroll
    for (int i = 0; i < 5; ++i) s = dot2(xs[i], 0x3f803f80u, s);
    const float mean = sum_halves(sum32(s)) * (1.0f / D_);
    f32x2 v2 = {0.f, 0.f};
#pragma unroll
    for (int i = 0; i < 5; ++i) { const f32x2 d = f32x2{bflo(xs[i]), bfhi(xs[i])} - mean; v2 += d * d; }
    const float sd = ln_sd(sum_halves(sum32(v2[0] + v2[1])) * (1.0f / D_));
    const float mb = rbf(mean), rc = 1.0f / sd;
#pragma unroll
    for (int i = 0; i < 5; ++i) *(LAS unsigned*)(smem + L_XS + i * 256 + lane * 4) = ln_pair(xs[i], mb, sd, rc);
  }
  DmaJob dj;
  if (w < 6) {
    const int ln = opaque(lane);
    const unsigned vo2 = w < 4 ? ln * (F_ * 2) : ln * 16;
#pragma unroll
    for (int i = 0; i < 5; ++i) dj.voff[i] = vo2;
    dj.sstr = w < 4 ? 64 * F_ * 2 : 1024;
    dj.sb = (w < 4 ? (const char*)((w < 2 ? wg : wu) + fcol) : (const char*)(wd + (size_t)fcol * D_)) + (size_t)(w & 1) * 5 * dj.sstr;
    dj.lds = lds0 + L_G + w * 5120;
    dj.on = has_ffn;
  } else {
    const int ln = opaque(lane);
    const char* qbase = (const char*)wo + hg * 160 + (size_t)(jj >> 3) * 204800 + (size_t)(w - 6) * 81920;
    touch4(qbase, (ln >> 1) * 1280 + (ln & 1) * 128, lds0 + L_FRED + 512);
    touch4(qbase + 40960, (ln >> 1) * 1280 + (ln & 1) * 128, lds0 + L_FRED + 512);
    if (w == 7) touch4(qbase + 81920, (ln >> 1) * 1280 + (ln & 1) * 128, lds0 + L_FRED + 512);
#pragma unroll
    for (int i = 0; i < 5; ++i) dj.voff[i] = 0;
    dj.sstr = 0; dj.sb = (const char*)wo; dj.lds = lds0; dj.on = 0;
  }
  u32x4 xe[2], ge[2];
  f32x4 yl[3];
  const bool sel_blk = (ridx >> 6) == qb;
#pragma unroll 1
  for (int attempt = 0;; ++attempt) {
#pragma unroll 1
    for (int st = 0; st < 8; st += 2) {
      int flag_off = -1;
      if (attempt == 0) {
        if (st == 4) {
          if (has_ffn && w < 4) {
            const int ln = opaque(lane);
            const int g = ln >> 4, q = (ln & 15) >> 2, pp = ln & 3;
            const char LAS* gb = smem + L_G + (pp >> 1) * 10240 + (8 * g + q) * 16 + (pp & 1) * 8 + w * 2560;
            const char LAS* xsp = smem + L_XS + w * 320 + g * 16;
            f32x4 a = {0, 0, 0, 0};
#pragma unroll
            for (int s = 0; s < 5; ++s)
              a = MFMA16(*(const LAS bf16x8*)(xsp + s * 64), cat8(trread(gb + s * 512), trread(gb + s * 512 + 64)), a);
            if (ln < 16) *(LAS float*)(smem + L_FRED + ln * 16 + w * 4) = a[0];
          }
        } else if (st == 6) {
          drain();
          if (has_ffn && w == 2) flag_off = WS_FY + pid * 128;
        }
      }
      if (attempt != 0 || st != 0)
        att_block(kv, smem, lane, ((w + st + 7) & 7) + 8, ((w + st + 1) & 7) + 8, (w + st + 1) & 7, q1, q2, sa, sb, p1, ob, oa,
                  [&](int i) { if (i == 5 && flag_off >= 0 && lane == 0) st4(rs, tag, flag_off); });
      unsigned waitmode = 0, yflag = 0;
      bool ylook = false;
      if (attempt == 0) {
        if (st == 2) {
          waitmode = 1;
          w_voff(opaque(lane), dj.voff);
          dj.sstr = 0;
          dj.sb = (const char*)wo + hg * 160 + (size_t)w * 40960; dj.lds = lds0 + L_W + w * 5120; dj.on = 1;
        } else if (st == 4) {
          dj.sb = (const char*)wo + hg * 160 + (size_t)(w < 2 ? 8 + w : 15 + w) * 40960;
          dj.lds = lds0 + (w < 2 ? L_W + (8 + w) * 5120 : L_WHI + (5 + w) * 5120); dj.on = w < 4;
          if (has_ffn && w >= 4) {
            const int ln = opaque(lane);
            const int cl = ln < 40 ? ln : 39;
            const f32x4 g4 = *(const LAS f32x4*)(smem + L_FRED + (ln & 15) * 16);
            u32x2 wv2[8];
#pragma unroll
            for (int c = 0; c < 8; ++c) wv2[c] = *(const LAS u32x2*)(smem + L_DN + c * 1280 + (w - 4) * 320 + cl * 8);
            const float gu = rbf((g4[0] + g4[1]) + (g4[2] + g4[3]));
            const float up = dpp_mov<0x128>(gu);
            const float hval = rbf(rbf(silu_f(gu)) * up);
            f32x4 y = {0, 0, 0, 0};
#pragma unroll
            for (int c = 0; c < 8; ++c) {
              const float hc = rdlane(hval, c);
              y[0] = fmaf(hc, bflo(wv2[c][0]), y[0]); y[1] = fmaf(hc, bfhi(wv2[c][0]), y[1]);
              y[2] = fmaf(hc, bflo(wv2[c][1]), y[2]); y[3] = fmaf(hc, bfhi(wv2[c][1]), y[3]);
            }
            if (ln < 40) st16(rs, __builtin_bit_cast(u32x4, y), ln * 16 + (ln >= 20 ? 2560 - 320 : 0),
                              WS_YP + (qb * 8 + (w - 4) * 2) * 2560 + hg * 320);
          }
        } else if (st == 6) {
          dj.sb = (const char*)wo + hg * 160 + (size_t)(w < 7 ? 10 + w : 19) * 40960;
          dj.lds = lds0 + L_WHI + (w < 7 ? w : 9) * 5120; dj.on = 1;
          if (has_ffn && w == 3) {
            asm volatile("global_load_dword %0, %1, %2 sc1" : "=v"(yflag) : "v"((lane & 7) * 128), "s"(ws + WS_FY + qb * 1024) : "memory");
            ylook = true;
          }
        }
      }
      att_block(kv, smem, lane, (w + st) & 7, (w + st + (st == 6 ? 1 : 2)) & 7, (w + st + 2) & 7, q1, q2, sa, sb, p1, oa, ob,
                [&](int i) {
                  dma_hook(dj, waitmode, i);
                  if (i == 3 && ylook) {
                    asm volatile("s_waitcnt vmcnt(0)" : "+v"(yflag) :: "memory");
                    if (!__all(yflag == tag)) poll_words(rs, (lane & 7) * 128, WS_FY + qb * 1024, tag);
                    CFENCE();
                    const int ln = opaque(lane);
#pragma unroll
                    for (int k = 0; k < 3; ++k)
                      yl[k] = __builtin_bit_cast(f32x4, ld16(rs, (ln < 60 && k * 60 + ln < 160) ? k * 960 + ln * 16 : 0x7ff00000, WS_YP + pid * 2560));
                  }
                });
      dj.on = 0;

    }
    __builtin_amdgcn_s_setprio(0);
    ob = MFMA32(kv.v[7][1], __builtin_bit_cast(bf16x8, p1), ob);
#pragma unroll
    for (int e = 0; e < 12; ++e) ob[e] += oa[e];

    const int tid4 = opaque(tid), ln4 = tid4 & 63;
    const float l = hi_half(ob[8]);
    const bool bad = attempt == 0 && __any(!(l >= 5.4e-20f && l <= 1e30f));
    LAS unsigned* ocnt = (LAS unsigned*)(smem + L_CNT);
    if (!bad) {
      const int b = (w + 7) & 7, hh = b >> 1, tok = qb * 64 + (b & 1) * 32 + (ln4 & 31), h4 = ln4 >> 5;
      const float inv = 1.0f / l;
      unsigned A0 = pk2(ob[0] * inv, ob[1] * inv), A1 = pk2(ob[2] * inv, ob[3] * inv);
      unsigned B0 = pk2(ob[4] * inv, ob[5] * inv), B1 = pk2(ob[6] * inv, ob[7] * inv);
      unsigned C0 = pk2(ob[8] * inv, ob[9] * inv), C1 = pk2(ob[10] * inv, ob[11] * inv);
      const int base = WS_O + ((tok >> 4) * 80 + hg * 10 + ((hh * 5) >> 1)) * 256 + (tok & 15) * 16;
      if ((hh & 1) == 0) {
        auto s0 = __builtin_amdgcn_permlane32_swap(A0, B0, false, false);
        auto s1 = __builtin_amdgcn_permlane32_swap(A1, B1, false, false);
        st16(rs, u32x4{s0[0], s1[0], s0[1], s1[1]}, base + h4 * 256);
        if (h4 == 0) st8(rs, u32x2{C0, C1}, base + 512);
      } else {
        auto s0 = __builtin_amdgcn_permlane32_swap(A0, C0, false, false);
        auto s1 = __builtin_amdgcn_permlane32_swap(A1, C1, false, false);
        st16(rs, h4 == 0 ? u32x4{s0[1], s1[1], B0, B1} : u32x4{B0, B1, s0[0], s1[0]}, base + 256 + h4 * 256);
        if (h4 == 0) st8(rs, u32x2{s0[0], s1[0]}, base + 8);
      }
    }
    const bool ysum = attempt == 0 && has_ffn && w == 3;
    if (ysum) {
      f32x4 a = (yl[0] + yl[1]) + yl[2];
#pragma unroll
      for (int e = 0; e < 4; ++e) a[e] = (a[e] + __shfl_down(a[e], 20)) + __shfl_down(a[e], 40);
      if (ln4 < 20) st16(rs, __builtin_bit_cast(u32x4, a), ln4 * 16, WS_YQ + (hg * 27 + qb) * 320);
    }
    drain();
    if (ln4 == 0 && lds_add_rtn(ocnt, bad ? 0x101u : 1u) == 7u && !bad) st4(rs, tag, WS_FC + pid * 128);
    if (ysum && ln4 == 0) st4(rs, tag, WS_FZ + hg * 4096 + qb * 128);

#pragma unroll
    for (int i = 0; i < 2; ++i) {
      int n = i * 512 + tid4; n = n < 640 ? n : 639;
      const int row = n / 10, ch = n - row * 10, tok = qb * 64 + row;
      xe[i] = *(const u32x4*)(x + (size_t)tok * D_ + hg * 80 + ch * 8);
    }
    if (w == 7) {
#pragma unroll 1
      for (unsigned spin = 0; spin < SPIN_CAP; ++spin) {
        CFENCE();
        const unsigned v = ld4(rs, (ln4 & 7) * 128, WS_FC + qb * 1024);
        if (__all(v == tag) || lds_get(ocnt) > 0xffu) break;
        __builtin_amdgcn_s_sleep(1);
      }
    }
    const bool selrow0 = qb * 64 + tid4 / 10 == ridx, selrow1 = qb * 64 + (512 + tid4) / 10 == ridx;
#pragma unroll
    for (int i = 0; i < 2; ++i) {
      const unsigned stp = i == 0 ? stp0 : stp1;
      const float mb = bflo(stp), sd = bfhi(stp), rc = 1.0f / sd;
#pragma unroll
      for (int e = 0; e < 4; ++e) ge[i][e] = ln_pair(xe[i][e], mb, sd, rc);
      if (!(i == 0 ? selrow0 : selrow1)) {
#pragma unroll
        for (int e = 0; e < 4; ++e) ge[i][e] = pk2(silu_f(bflo(ge[i][e])), silu_f(bfhi(ge[i][e])));
      }
    }
    bar_all();
    if (lds_get(ocnt) <= 0xffu) break;
    {
      const int c32 = ln4 & 31, h = ln4 >> 5;
#pragma unroll 1
      for (int b = 0; b < 8; ++b) {
        const bf16x8 fq1 = *(const LAS bf16x8*)(smem + L_Q + b * 1536 + ln4 * 16);
        s16x4 fq2 = *(const LAS s16x4*)(smem + L_Q + b * 1536 + 1024 + ln4 * 8);
        if (h == 1) fq2 = s16x4{0, 0, 0, 0};
        float mx = -INFINITY;
#pragma unroll
        for (int t = 0; t < 8; ++t) {
          f32x16 s = MFMA32(kv.k1[t], fq1, zero16);
          s = MFMA32K8(kv.k2[t], fq2, s);
#pragma unroll
          for (int e = 0; e < 16; ++e) mx = fmaxf(mx, s[e]);
        }
        mx = fmaxf(mx, __shfl_xor(mx, 32));
        if (h == 0) *(LAS float*)(smem + L_MAXS + (w * 256 + b * 32 + c32) * 4) = mx;
      }
      bar_lds();
      if (tid4 < 256) {
        float mx = -INFINITY;
#pragma unroll
        for (int i = 0; i < 8; ++i) mx = fmaxf(mx, *(LAS float*)(smem + L_MAXS + (i * 256 + tid4) * 4));
        const float hi = rbf(mx), lo = mx - hi;
        *(LAS u32x2*)(smem + L_Q + (tid4 >> 5) * 1536 + 1024 + ((tid4 & 31) + 32) * 8) = u32x2{pk2(-hi, -lo), 0u};
      }
      bar_lds();
#pragma unroll
      for (int i = 0; i < 6; ++i) *(LAS f32x4*)(smem + L_RING + i * 8192 + tid4 * 16) = f32x4{0, 0, 0, 0};
      bar_lds();
      q1 = *(const LAS bf16x8*)(smem + L_Q + w * 1536 + lane * 16);
      q2 = *(const LAS s16x4*)(smem + L_Q + w * 1536 + 1024 + lane * 8);
      sa = MFMA32(kv.k1[0], q1, zero16);
      sa = MFMA32K8(kv.k2[0], q2, sa);
      p1 = u32x4{0, 0, 0, 0};
      oa = zero16; ob = zero16;
    }
  }

  const int tid5 = opaque(tid), ln5 = tid5 & 63;
#pragma unroll
  for (int s = 0; s < 5; ++s)
    af[s] = __builtin_bit_cast(bf16x8, ld16(rs, ln5 * 16 + s * 1024, WS_O + ((qb * 4 + mt) * 20 + kh * 10) * 1024));
  LAS float* ys = (LAS float*)(smem + L_YS);
  {
    f32x4 acc[5] = {{0, 0, 0, 0}, {0, 0, 0, 0}, {0, 0, 0, 0}, {0, 0, 0, 0}, {0, 0, 0, 0}};
    gemm_half(af, smem + (kh ? L_WHI : L_W), ln5, acc, [&](int t, const bf16x8 (&)[5]) {
      if (t < 5) af[t + 5] = __builtin_bit_cast(bf16x8, ld16(rs, ln5 * 16 + (t + 5) * 1024, WS_O + ((qb * 4 + mt) * 20 + kh * 10) * 1024));
    });
    write_part(smem + L_PART2, kh, mt, ln5, acc);
  }

  f32x4 yt[9];
  if (sel_blk && w == 0) {
    poll_words(rs, (ln5 < 27 ? ln5 : 26) * 128, WS_FZ + hg * 4096, tag);
#pragma unroll
    for (int i = 0; i < 9; ++i) yt[i] = __builtin_bit_cast(f32x4, ld16(rs, (ln5 < 60 ? ln5 : 59) * 16 + i * 960, WS_YQ + hg * 8640));
  }
  bar_lds();
  auto finish = [&](int i, bool sel_pass) {
    const int n = i * 512 + tid5;
    const int row = n / 10, ch = n - row * 10, tok = qb * 64 + row;
    if (n < 640 && (tok == ridx) == sel_pass) {
      const char LAS* pp = smem + L_PART2 + row * 336 + ch * 32;
      const f32x4 s0 = *(const LAS f32x4*)(pp) + *(const LAS f32x4*)(pp + 21504);
      const f32x4 s1 = *(const LAS f32x4*)(pp + 16) + *(const LAS f32x4*)(pp + 21504 + 16);
      if (sel_pass) {
#pragma unroll
        for (int e = 0; e < 4; ++e)
          ge[i][e] = pk2(silu_f(rbf(bflo(ge[i][e]) + ys[ch * 8 + 2 * e])), silu_f(rbf(bfhi(ge[i][e]) + ys[ch * 8 + 2 * e + 1])));
      }
      u32x4 r;
      r[0] = pk2(bflo(xe[i][0]) + rbf(rbf(s0[0]) * bflo(ge[i][0])), bfhi(xe[i][0]) + rbf(rbf(s0[1]) * bfhi(ge[i][0])));
      r[1] = pk2(bflo(xe[i][1]) + rbf(rbf(s0[2]) * bflo(ge[i][1])), bfhi(xe[i][1]) + rbf(rbf(s0[3]) * bfhi(ge[i][1])));
      r[2] = pk2(bflo(xe[i][2]) + rbf(rbf(s1[0]) * bflo(ge[i][2])), bfhi(xe[i][2]) + rbf(rbf(s1[1]) * bfhi(ge[i][2])));
      r[3] = pk2(bflo(xe[i][3]) + rbf(rbf(s1[2]) * bflo(ge[i][3])), bfhi(xe[i][3]) + rbf(rbf(s1[3]) * bfhi(ge[i][3])));
      *(u32x4*)(out + (size_t)tok * D_ + hg * 80 + ch * 8) = r;
    }
  };
#pragma unroll 1
  for (int pass = 0; pass < 2; ++pass) {
    finish(0, pass == 1);
    finish(1, pass == 1);
    if (!sel_blk || pass == 1) break;
    if (w == 0) {
      f32x4 a = yt[0];
#pragma unroll
      for (int i = 1; i < 9; ++i) a += yt[i];
      const float rsig = rbf(sigm_f(rbf(rmax)));
#pragma unroll
      for (int e = 0; e < 4; ++e) a[e] = rbf(rbf((a[e] + __shfl_down(a[e], 20)) + __shfl_down(a[e], 40)) * rsig);
      if (ln5 < 20) *(LAS f32x4*)(ys + ln5 * 4) = a;
    }
    bar_lds();
  }

}

extern "C" void kernel_launch(void* const* d_in, const int* in_sizes, int n_in, void* d_out, int out_size,
                              void* d_ws, size_t ws_size, hipStream_t stream) {
  fused<<<256, 512, 0, stream>>>((const u16*)d_in[0], (const u16*)d_in[1], (const u16*)d_in[2], (const u16*)d_in[3],
                                 (const u16*)d_in[4], (const u16*)d_in[6], (const u16*)d_in[7], (const u16*)d_in[8],
                                 (const u16*)d_in[9], (char*)d_ws, (u16*)d_out);
}
```

```cpp
#include <hip/hip_runtime.h>
#include <hip/hip_bf16.h>
#include <stdint.h>

typedef __attribute__((ext_vector_type(8))) __bf16 bf16x8;
typedef __attribute__((ext_vector_type(2))) __bf16 bf16x2;
typedef __attribute__((ext_vector_type(4))) short s16x4;
typedef __attribute__((ext_vector_type(2))) float f32x2;
typedef __attribute__((ext_vector_type(4))) float f32x4;
typedef __attribute__((ext_vector_type(16))) float f32x16;
typedef __attribute__((ext_vector_type(2))) unsigned u32x2;
typedef __attribute__((ext_vector_type(4))) unsigned u32x4;
typedef unsigned short u16;
#define LAS __attribute__((address_space(3)))
#define DI __device__ __forceinline__
#define SB() __builtin_amdgcn_sched_barrier(0)
#define CFENCE() asm volatile("" ::: "memory")

constexpr int S_ = 2048, D_ = 640, F_ = 1728;

constexpr int WS_O = 0;
constexpr int WS_KF1 = WS_O + S_ * D_ * 2;
constexpr int WS_KF2 = WS_KF1 + 65536;
constexpr int WS_VF = WS_KF2 + 64 * 272;
constexpr int WS_YP = WS_VF + 64 * 1408;
constexpr int WS_YQ = WS_YP + 216 * D_ * 4;
constexpr int WS_FC = WS_YQ + 216 * 320 + 128 * 4;
constexpr int WS_FB = WS_FC + 32768;
constexpr int WS_FY = WS_FB + 32768;
constexpr int WS_FZ = WS_FY + 32768;
constexpr int WS_EP = WS_FZ + 32768;
static_assert(WS_FC % 128 == 0, "flag lines");
constexpr int WS_BYTES = WS_EP + 256;

constexpr int L_W = 0;
constexpr int L_SCR = 101376;
constexpr int L_WK = 102400, L_WV = 128000, L_X = 153600;
constexpr int L_WHI = 112640;
constexpr int L_RING = 51200;
constexpr int L_Q = 100352;
constexpr int L_PART = 112640;
constexpr int L_G = 112640, L_U = 122880, L_DN = 133120;
constexpr int L_FRED = 143360;
constexpr int L_XS = 161280;
constexpr int L_SUM = 162560;
constexpr int L_STAT = 155648;
constexpr int L_CS = 156672;
constexpr int L_STP = 159232;
constexpr int L_MAXS = L_RING;
constexpr int L_PART2 = L_RING;
constexpr int L_XSUM = L_RING + 43008;
constexpr int L_YS = L_RING + 48768;
constexpr int L_CNT = L_Q + 1024 + 256 + 4;
constexpr int LDS_BYTES = 163840;

constexpr float QSCALE = 0.22360679774997896f * 1.4426950408889634f;
constexpr unsigned SPIN_CAP = 1u << 18;
#define POLL_SLEEP 4

DI float bflo(unsigned v) { return __uint_as_float(v << 16); }
DI float bfhi(unsigned v) { return __uint_as_float(v & 0xffff0000u); }
DI unsigned pk2(float a, float b) {
  f32x2 t = {a, b};
  return __builtin_bit_cast(unsigned, __builtin_convertvector(t, bf16x2));
}
DI float rbf(float a) { return bflo(pk2(a, a)); }
DI float dot2(unsigned a, unsigned b, float c) {
  return __builtin_amdgcn_fdot2_f32_bf16(__builtin_bit_cast(bf16x2, a), __builtin_bit_cast(bf16x2, b), c, false);
}
DI s16x4 trread(const char LAS* p) {
  return __builtin_bit_cast(s16x4, __builtin_amdgcn_ds_read_tr16_b64_v4i16((LAS s16x4*)p));
}
DI bf16x8 cat8(s16x4 a, s16x4 b) {
  return __builtin_bit_cast(bf16x8, __builtin_shufflevector(a, b, 0, 1, 2, 3, 4, 5, 6, 7));
}
#define MFMA16(a, b, c) __builtin_amdgcn_mfma_f32_16x16x32_bf16((a), (b), (c), 0, 0, 0)
#define MFMA32(a, b, c) __builtin_amdgcn_mfma_f32_32x32x16_bf16((a), (b), (c), 0, 0, 0)
#define MFMA32K8(a, b, c) __builtin_amdgcn_mfma_f32_32x32x8bf16_1k((a), (b), (c), 0, 0, 0)
#define EXP2(x) __builtin_amdgcn_exp2f(x)
DI float sigm_f(float v) { return __builtin_amdgcn_rcpf(1.0f + EXP2(v * -1.4426950408889634f)); }
DI float silu_f(float v) { return v * sigm_f(v); }
DI void bar_lds() { asm volatile("s_waitcnt lgkmcnt(0)\n\ts_barrier" ::: "memory"); }
DI void bar_all() { asm volatile("s_waitcnt vmcnt(0) lgkmcnt(0)\n\ts_barrier" ::: "memory"); }
DI void drain() { asm volatile("s_waitcnt vmcnt(0)" ::: "memory"); }
DI void lds_add(LAS unsigned* p, unsigned v) {
  asm volatile("s_waitcnt lgkmcnt(0)" ::: "memory");
  __hip_atomic_fetch_add(p, v, __ATOMIC_RELAXED, __HIP_MEMORY_SCOPE_WORKGROUP);
}
DI unsigned lds_add_rtn(LAS unsigned* p, unsigned v) {
  asm volatile("s_waitcnt lgkmcnt(0)" ::: "memory");
  const unsigned r = __hip_atomic_fetch_add(p, v, __ATOMIC_RELAXED, __HIP_MEMORY_SCOPE_WORKGROUP);
  CFENCE();
  return r;
}
DI unsigned lds_get(LAS unsigned* p) {
  CFENCE();
  const unsigned r = __builtin_amdgcn_readfirstlane(__hip_atomic_load(p, __ATOMIC_RELAXED, __HIP_MEMORY_SCOPE_WORKGROUP));
  CFENCE();
  return r;
}
DI void lds_wait(LAS unsigned* p, unsigned target) {
#pragma unroll 1
  while (lds_get(p) < target) __builtin_amdgcn_s_sleep(1);
}
DI void dma16(const void* sbase, unsigned voff, unsigned lds) {
  unsigned keep;
  asm volatile("s_mov_b32 %0, m0\n\ts_mov_b32 m0, %3\n\ts_nop 3\n\tglobal_load_lds_dwordx4 %1, %2\n\ts_mov_b32 m0, %0"
               : "=&s"(keep) : "v"(voff), "s"(sbase), "s"(lds) : "memory");
}
DI void dma16_if(unsigned on, const void* sbase, unsigned voff, unsigned lds) {
  unsigned keep;
  on = __builtin_amdgcn_readfirstlane(on);
  lds = __builtin_amdgcn_readfirstlane(lds);
  sbase = (const void*)(((unsigned long long)__builtin_amdgcn_readfirstlane((unsigned)((unsigned long long)sbase >> 32)) << 32) |
                        (unsigned)__builtin_amdgcn_readfirstlane((unsigned)(unsigned long long)sbase));
  asm volatile("s_cmp_eq_u32 %4, 0\n\ts_cbranch_scc1 1f\n\ts_mov_b32 %0, m0\n\ts_mov_b32 m0, %3\n\ts_nop 1\n\t"
               "global_load_lds_dwordx4 %1, %2\n\ts_mov_b32 m0, %0\n1:"
               : "=&s"(keep) : "v"(voff), "s"(sbase), "s"(lds), "s"(on) : "memory", "scc");
}
DI void touch4(const void* sbase, unsigned voff, unsigned lds) {
  unsigned keep;
  asm volatile("s_mov_b32 %0, m0\n\ts_mov_b32 m0, %3\n\ts_nop 3\n\tglobal_load_lds_dword %1, %2\n\ts_mov_b32 m0, %0"
               : "=&s"(keep) : "v"(voff), "s"(sbase), "s"(lds) : "memory");
}
typedef __amdgpu_buffer_rsrc_t rsrc_t;
DI u32x4 ld16(rsrc_t r, int v, int s = 0) { return __builtin_amdgcn_raw_buffer_load_b128(r, v, s, 16); }
DI u32x2 ld8(rsrc_t r, int v, int s = 0) { return __builtin_amdgcn_raw_buffer_load_b64(r, v, s, 16); }
DI unsigned ld4(rsrc_t r, int v, int s = 0) { return __builtin_amdgcn_raw_buffer_load_b32(r, v, s, 16); }
DI void st16(rsrc_t r, u32x4 d, int v, int s = 0) { __builtin_amdgcn_raw_buffer_store_b128(d, r, v, s, 16); }
DI void st8(rsrc_t r, u32x2 d, int v, int s = 0) { __builtin_amdgcn_raw_buffer_store_b64(d, r, v, s, 16); }
DI void st4(rsrc_t r, unsigned d, int v, int s = 0) { __builtin_amdgcn_raw_buffer_store_b32(d, r, v, s, 16); }

template <int CTRL> DI float dpp_mov(float v) {
  return __builtin_bit_cast(float, __builtin_amdgcn_update_dpp(0, __builtin_bit_cast(int, v), CTRL, 0xf, 0xf, true));
}
DI float sum16(float v) {
  v += dpp_mov<0xB1>(v); v += dpp_mov<0x4E>(v); v += dpp_mov<0x141>(v); v += dpp_mov<0x140>(v);
  return v;
}
DI float sum_rows(float v) {
  const unsigned b = __float_as_uint(v);
  auto r = __builtin_amdgcn_permlane16_swap(b, b, false, false);
  return __uint_as_float(r[0]) + __uint_as_float(r[1]);
}
DI float sum_halves(float v) {
  const unsigned b = __float_as_uint(v);
  auto r = __builtin_amdgcn_permlane32_swap(b, b, false, false);
  return __uint_as_float(r[0]) + __uint_as_float(r[1]);
}
DI float hi_half(float v) {
  const unsigned b = __float_as_uint(v);
  auto r = __builtin_amdgcn_permlane32_swap(b, b, false, false);
  return __uint_as_float(r[1]);
}
DI float max64(float v) {
  v = fmaxf(v, dpp_mov<0xB1>(v)); v = fmaxf(v, dpp_mov<0x4E>(v)); v = fmaxf(v, dpp_mov<0x141>(v)); v = fmaxf(v, dpp_mov<0x140>(v));
  auto r = __builtin_amdgcn_permlane16_swap(__float_as_uint(v), __float_as_uint(v), false, false);
  v = fmaxf(__uint_as_float(r[0]), __uint_as_float(r[1]));
  auto q = __builtin_amdgcn_permlane32_swap(__float_as_uint(v), __float_as_uint(v), false, false);
  return fmaxf(__uint_as_float(q[0]), __uint_as_float(q[1]));
}
DI int opaque(int v) { asm volatile("" : "+v"(v)); return v; }
DI float sum32(float v) { return sum_rows(sum16(v)); }
DI float rdlane(float v, int l) { return __builtin_bit_cast(float, __builtin_amdgcn_readlane(__builtin_bit_cast(int, v), l)); }

DI float ln_sd(float var) { return rbf(sqrtf(rbf(rbf(var) + 1e-5f))); }
DI unsigned ln_pair(unsigned xp, float mb, float sd, float rc) {
  const unsigned dp = pk2(bflo(xp) - mb, bfhi(xp) - mb);
  const f32x2 d = {bflo(dp), bfhi(dp)};
  const f32x2 q0 = d * rc;
  const f32x2 a = __builtin_elementwise_fma(__builtin_elementwise_fma(-q0, f32x2{sd, sd}, d), f32x2{rc, rc}, q0);
  return pk2(a[0], a[1]);
}
DI void load_piece(const u16* __restrict__ xrow0, int lane, u32x4 (&xp)[10]) {
#pragma unroll
  for (int i = 0; i < 5; ++i) {
    const int n = i * 64 + lane, row = n / 20, ch = n - row * 20;
    xp[i] = *(const u32x4*)((const char*)xrow0 + row * 1280 + ch * 16);
    xp[5 + i] = *(const u32x4*)((const char*)xrow0 + row * 1280 + ch * 16 + 320);
  }
}
DI void bounce_piece(char LAS* blk, int lane, const u32x4 (&xp)[10], bf16x8 (&af)[10]) {
#pragma unroll
  for (int hf = 0; hf < 2; ++hf) {
#pragma unroll
    for (int i = 0; i < 5; ++i) {
      const int n = i * 64 + lane, row = n / 20, ch = n - row * 20;
      *(LAS u32x4*)(blk + row * 336 + ch * 16) = xp[hf * 5 + i];
    }
#pragma unroll
    for (int s = 0; s < 5; ++s) af[hf * 5 + s] = *(const LAS bf16x8*)(blk + (lane & 15) * 336 + (lane >> 4) * 16 + s * 64);
  }
}
DI void w_voff(int lane, unsigned (&voff)[5]) {
#pragma unroll
  for (int i = 0; i < 5; ++i) {
    const int n = i * 64 + lane, p = n / 10, ch = n - p * 10;
    const int k = (p & ~12) | ((p & 4) << 1) | ((p & 8) >> 1);
    voff[i] = k * 1280 + ch * 16;
  }
}
template <class F>
DI void gemm_half(bf16x8 (&af)[10], const char LAS* wimg, int lane, f32x4 (&acc)[5], F side) {
  const int g = lane >> 4, q = (lane & 15) >> 2, pp = lane & 3;
  const char LAS* wb = wimg + (16 * (g >> 1) + 4 * (g & 1) + q) * 160 + 8 * pp;
  bf16x8 wf[3][5];
#pragma unroll
  for (int p = 0; p < 2; ++p)
#pragma unroll
    for (int i = 0; i < 5; ++i) wf[p][i] = cat8(trread(wb + p * 5120 + i * 32), trread(wb + p * 5120 + 1280 + i * 32));
#pragma unroll
  for (int s = 0; s < 10; ++s) {
#pragma unroll
    for (int i = 0; i < 5; ++i) {
      acc[i] = MFMA16(wf[s % 3][i], af[s], acc[i]);
      SB();
      if (s < 8) wf[(s + 2) % 3][i] = cat8(trread(wb + (s + 2) * 5120 + i * 32), trread(wb + (s + 2) * 5120 + 1280 + i * 32));
      SB();
    }
    side(s, wf[s % 3]);
    SB();
  }
}
DI void write_part(char LAS* part, int kh, int mt, int lane, const f32x4 (&acc)[5]) {
  char LAS* p = part + kh * 21504 + (mt * 16 + (lane & 15)) * 336 + (lane >> 4) * 16;
#pragma unroll
  for (int i = 0; i < 5; ++i) *(LAS f32x4*)(p + i * 64) = acc[i];
}

struct KVRegs { bf16x8 k1[8]; s16x4 k2[8]; bf16x8 v[8][2]; };
struct DmaJob { const char* sb; unsigned sstr, lds, on; unsigned voff[5]; };

#define EXP_PK(dst, S, b) { dst[0] = pk2(EXP2(S[b]), EXP2(S[b + 1])); dst[1] = pk2(EXP2(S[b + 2]), EXP2(S[b + 3])); }
template <bool NEWBLK, int PRIO>
DI void att_step(f32x16& sc, f32x16& sn, const bf16x8& k1n, const s16x4& k2n, const bf16x8& q1n, const s16x4& q2n,
                 const bf16x8& v0, const bf16x8& v1prev, u32x4& p1, f32x16& oprev, f32x16& ocur) {
  const f32x16 zero16 = {0, 0, 0, 0, 0, 0, 0, 0, 0, 0, 0, 0, 0, 0, 0, 0};
  u32x2 a, b, c, d;
  if (PRIO >= 0) __builtin_amdgcn_s_setprio(PRIO);
  sn = MFMA32(k1n, q1n, zero16); SB();
  EXP_PK(a, sc, 0); SB();
  sn = MFMA32K8(k2n, q2n, sn); SB();
  EXP_PK(b, sc, 4); SB();
  oprev = MFMA32(v1prev, __builtin_bit_cast(bf16x8, p1), oprev); SB();
  EXP_PK(c, sc, 8); SB();
  const u32x4 p0 = {a[0], a[1], b[0], b[1]};
  if (NEWBLK) ocur = MFMA32(v0, __builtin_bit_cast(bf16x8, p0), ocur);
  else ocur = MFMA32(v0, __builtin_bit_cast(bf16x8, p0), oprev);
  SB();
  EXP_PK(d, sc, 12); SB();
  p1 = u32x4{c[0], c[1], d[0], d[1]};
}
template <class H>
DI void att_block(const KVRegs& kv, char LAS* smem, int lane, int sout, int sin, int bnext, bf16x8& q1, s16x4& q2,
                  f32x16& sa, f32x16& sb, u32x4& p1, f32x16& oprev, f32x16& ocur, H hook) {
  LAS f32x4* so = (LAS f32x4*)(smem + L_RING + sout * 3072) + lane;
  LAS f32x4* si = (LAS f32x4*)(smem + L_RING + sin * 3072) + lane;
  att_step<true, -1>(sa, sb, kv.k1[1], kv.k2[1], q1, q2, kv.v[0][0], kv.v[7][1], p1, oprev, ocur);
  hook(0); SB();
  att_step<false, 1>(sb, sa, kv.k1[2], kv.k2[2], q1, q2, kv.v[1][0], kv.v[0][1], p1, ocur, ocur);
  so[0] = f32x4{oprev[0], oprev[1], oprev[2], oprev[3]};
  hook(1); SB();
  att_step<false, -1>(sa, sb, kv.k1[3], kv.k2[3], q1, q2, kv.v[2][0], kv.v[1][1], p1, ocur, ocur);
  so[64] = f32x4{oprev[4], oprev[5], oprev[6], oprev[7]};
  hook(2); SB();
  att_step<false, 0>(sb, sa, kv.k1[4], kv.k2[4], q1, q2, kv.v[3][0], kv.v[2][1], p1, ocur, ocur);
  so[128] = f32x4{oprev[8], oprev[9], oprev[10], oprev[11]};
  hook(3); SB();
  att_step<false, -1>(sa, sb, kv.k1[5], kv.k2[5], q1, q2, kv.v[4][0], kv.v[3][1], p1, ocur, ocur);
  hook(4);
  bar_lds();
  const bf16x8 q1n = *(const LAS bf16x8*)(smem + L_Q + bnext * 1536 + lane * 16);
  const s16x4 q2n = *(const LAS s16x4*)(smem + L_Q + bnext * 1536 + 1024 + lane * 8);
  SB();
  att_step<false, 3>(sb, sa, kv.k1[6], kv.k2[6], q1, q2, kv.v[5][0], kv.v[4][1], p1, ocur, ocur);
  {
    const f32x4 i0 = si[0], i1 = si[64], i2 = si[128];
#pragma unroll
    for (int e = 0; e < 4; ++e) { oprev[e] = i0[e]; oprev[4 + e] = i1[e]; oprev[8 + e] = i2[e]; }
  }
  hook(5); SB();
  att_step<false, -1>(sa, sb, kv.k1[7], kv.k2[7], q1, q2, kv.v[6][0], kv.v[5][1], p1, ocur, ocur);
  hook(6); SB();
  att_step<false, 2>(sb, sa, kv.k1[0], kv.k2[0], q1n, q2n, kv.v[7][0], kv.v[6][1], p1, ocur, ocur);
  q1 = q1n; q2 = q2n;
}
DI void dma_hook(const DmaJob& dj, unsigned waitmode, int i) {
  if (i == 4) {
    asm volatile("s_cmp_eq_u32 %0, 0\n\ts_cbranch_scc1 1f\n\ts_waitcnt vmcnt(3)\n\ts_cmp_eq_u32 %0, 1\n\ts_cbranch_scc1 1f\n\t"
                 "s_waitcnt vmcnt(0)\n1:" :: "s"(__builtin_amdgcn_readfirstlane(waitmode)) : "memory", "scc");
  } else if (i != 1) {
    const int c = i == 0 ? 0 : (i < 4 ? i - 1 : i - 2);
    dma16_if(dj.on, dj.sb + (size_t)c * dj.sstr, dj.voff[c], dj.lds + c * 1024);
  }
}
DI void poll_words(rsrc_t rs, int voff, int soff, unsigned tag) {
#pragma unroll 1
  for (unsigned spin = 0; spin < SPIN_CAP; ++spin) {
    CFENCE();
    const unsigned v = ld4(rs, voff, soff);
    if (__all(v == tag)) break;
    __builtin_amdgcn_s_sleep(1);
  }
  CFENCE();
}

__global__ __launch_bounds__(512, 2) void fused(const u16* __restrict__ x, const u16* __restrict__ wq,
                                                const u16* __restrict__ wk, const u16* __restrict__ wv,
                                                const u16* __restrict__ wo, const u16* __restrict__ wr,
                                                const u16* __restrict__ wg, const u16* __restrict__ wu,
                                                const u16* __restrict__ wd, char* ws, u16* __restrict__ out) {
  __shared__ __attribute__((aligned(16))) char smem_[LDS_BYTES];
  char LAS* smem = (char LAS*)smem_;
  const unsigned lds0 = (unsigned)(size_t)smem;
  const int tid = threadIdx.x, lane = tid & 63;
  const int w = __builtin_amdgcn_readfirstlane(tid >> 6);
  {
    const char* pc = (const char*)__builtin_amdgcn_s_getpc();
    touch4(pc + w * 4352, (lane < 34 ? lane : 33) * 128, lds0 + L_SCR + 512);
  }
  const int bid = blockIdx.x, xcd = bid & 7, jj = bid >> 3;
  const int qb = xcd * 4 + (jj >> 3), hg = jj & 7, pid = qb * 8 + hg;
  const bool has_ffn = qb < 27;
  const int fcol = pid * 8;
  const int tok0 = pid * 8, tile = tok0 >> 5;
  const rsrc_t rs = __builtin_amdgcn_make_buffer_rsrc(ws, 0, WS_BYTES, 0x00020000);
  LAS float* scr = (LAS float*)(smem + L_SCR);
  LAS unsigned* cnt = (LAS unsigned*)(smem + L_SCR + 96);

  unsigned tag = 0;
  u32x4 late = {0, 0, 0, 0};
  const int mt = w & 3, kh = w >> 2;
  bf16x8 af[10];
  const u16* xpiece = x + (size_t)(qb * 64 + mt * 16) * D_ + kh * 320;
  if (w < 4) {
    const int half = lane >> 5, l32 = lane & 31, tl = 2 * w + half;
    u32x2 xv[5], wv5[5];
    u32x4 xp[10];
#pragma unroll
    for (int i = 0; i < 5; ++i) xv[i] = *(const u32x2*)(x + (size_t)(tok0 + tl) * D_ + i * 128 + l32 * 4);
    const unsigned ep = ld4(rs, WS_EP);
#pragma unroll
    for (int i = 0; i < 5; ++i) wv5[i] = *(const u32x2*)(wr + i * 128 + l32 * 4);
    if (tid < 2) *((LAS u32x4*)cnt + tid) = u32x4{0, 0, 0, 0};
    bar_lds();
    load_piece(xpiece, lane, xp);
    SB();
    float s = 0.f;
#pragma unroll
    for (int i = 0; i < 5; ++i) { s = dot2(xv[i][0], 0x3f803f80u, s); s = dot2(xv[i][1], 0x3f803f80u, s); }

    const float mean = sum32(s) * (1.0f / D_);
    f32x2 v2 = {0.f, 0.f};
#pragma unroll
    for (int i = 0; i < 10; ++i) {
      const f32x2 d = f32x2{bflo(xv[i >> 1][i & 1]), bfhi(xv[i >> 1][i & 1])} - mean;
      v2 += d * d;
    }
    const float sd = ln_sd(sum32(v2[0] + v2[1]) * (1.0f / D_));
    const float mb = rbf(mean), rc = 1.0f / sd;
#pragma unroll
    for (int i = 0; i < 5; ++i) {
      xv[i] = u32x2{ln_pair(xv[i][0], mb, sd, rc), ln_pair(xv[i][1], mb, sd, rc)};
      *(LAS u32x2*)(smem + L_X + tl * 1280 + (((i * 16 + (l32 >> 1)) ^ tl) * 16) + (l32 & 1) * 8) = xv[i];
    }
    if (lane == 0) lds_add(cnt + 0, 1);
    float racc = 0.f;
#pragma unroll
    for (int i = 0; i < 5; ++i) { racc = dot2(xv[i][0], wv5[i][0], racc); racc = dot2(xv[i][1], wv5[i][1], racc); }
    racc = sum32(racc);
    if (l32 == 0) scr[tl] = racc;
    tag = __builtin_amdgcn_readfirstlane(ep) + 1u;
    lds_wait(cnt + 0, 6);

    const int g = lane >> 4, c = lane & 15, q = c >> 2, pp = lane & 3, r8 = c & 7;
    const int mat = w >> 1, nt = w & 1;
    const char LAS* wb = smem + L_WK + mat * 25600 + (8 * g + q) * 40 + nt * 32 + 8 * pp;
    const char LAS* xb0 = smem + L_X + r8 * 1280 + (g ^ r8) * 16;
    const char LAS* xb1 = smem + L_X + r8 * 1280 + ((4 + g) ^ r8) * 16;
    bf16x8 wf[20], xf[20];
#pragma unroll
    for (int s2 = 0; s2 < 20; ++s2) {
      xf[s2] = *(const LAS bf16x8*)(((s2 & 1) ? xb1 : xb0) + (s2 >> 1) * 128);
      wf[s2] = cat8(trread(wb + s2 * 1280), trread(wb + s2 * 1280 + 160));
    }
    f32x4 a0 = {0, 0, 0, 0}, a1 = {0, 0, 0, 0};
    if (mat == 0) {
#pragma unroll
      for (int s2 = 0; s2 < 20; s2 += 2) { a0 = MFMA16(wf[s2], xf[s2], a0); a1 = MFMA16(wf[s2 + 1], xf[s2 + 1], a1); }
    } else {
#pragma unroll
      for (int s2 = 0; s2 < 20; s2 += 2) { a0 = MFMA16(xf[s2], wf[s2], a0); a1 = MFMA16(xf[s2 + 1], wf[s2 + 1], a1); }
    }
    if (lane == 0) lds_add(cnt + 2, 1);
    a0 += a1;
    u32x2 pk = {pk2(a0[0], a0[1]), pk2(a0[2], a0[3])};
    int soff; bool son;
    if (mat == 0) {
      float n2 = dot2(pk[0], pk[0], dot2(pk[1], pk[1], 0.f));
      const int r = (tok0 & 31) + c;
      if (nt == 0) {
        soff = WS_KF1 + tile * 1024 + (r + 32 * (g >> 1)) * 16 + (g & 1) * 8; son = c < 8;
        n2 = sum_halves(sum_rows(n2));
      } else {
        if (lane == 8) pk = u32x2{0x3f803f80u, 0u};
        soff = WS_KF2 + tile * 272 + (lane < 8 ? r : 32) * 8; son = lane <= 8;
      }
      if (lane < 8) scr[8 + nt * 8 + lane] = n2;
    } else {
      const int d = 16 * nt + c, u = (tok0 & 31) >> 3;
      if (d == 20) pk = u32x2{0x3f803f80u, 0x3f803f80u};
      if (d == 21) pk = u32x2{0u, 0u};
      soff = WS_VF + ((tile * 2 + (u >> 1)) * 2 + g) * 352 + d * 16 + (u & 1) * 8; son = g < 2 && d < 22;
    }
    if (son) st8(rs, pk, soff);
    lds_wait(cnt + 2, 4);
    bounce_piece(smem + L_WK + w * 5376, lane, xp, af);
    drain();
    if (lane == 0 && lds_add_rtn(cnt + 3, 1) == 3) {
      f32x4 sv[6];
#pragma unroll
      for (int i = 0; i < 6; ++i) sv[i] = *(const LAS f32x4*)(scr + 4 * i);
      float rm = sv[0][0], km = sv[2][0] + sv[4][0]; int ri = 0;
#pragma unroll
      for (int i = 1; i < 8; ++i) {
        const float rr = sv[i >> 2][i & 3];
        const bool t = rr > rm;
        rm = t ? rr : rm; ri = t ? i : ri;
        km = fmaxf(km, sv[2 + (i >> 2)][i & 3] + sv[4 + (i >> 2)][i & 3]);
      }
      const unsigned kb = (__float_as_uint(km) + 0xffffu) & 0xffff0000u;
      st16(rs, u32x4{tag, __float_as_uint(rm), tag, kb | (unsigned)(tok0 + ri)}, WS_FB + pid * 128);
    }

    lds_wait(cnt + 4, 4);
    {
      unsigned voff[5];
      w_voff(lane, voff);
#pragma unroll
      for (int c = 0; c < 5; ++c)
        dma16((const char*)wq + hg * 160 + (size_t)w * 40960, voff[c], lds0 + L_W + w * 5120 + c * 1024);
    }
  } else {
    u32x4 xp[10];
    unsigned voff[5];
    if (w < 6) {
      u32x4 a[25];
      const char* src = (const char*)(w == 4 ? wk : wv) + lane * 16;
      const int rot = ((jj + xcd * 3) % 5) * 5;
#pragma unroll
      for (int i = 0; i < 25; ++i) {
        a[i] = *(const u32x4*)(src + (i + rot >= 25 ? i + rot - 25 : i + rot) * 1024);
        if (i == 0) bar_lds();
      }
      w_voff(lane, voff);
      SB();
      char LAS* dst = smem + (w == 4 ? L_WK : L_WV) + lane * 16;
#pragma unroll
      for (int i = 0; i < 25; ++i) *(LAS u32x4*)(dst + (i + rot >= 25 ? i + rot - 25 : i + rot) * 1024) = a[i];
      if (lane == 0) lds_add(cnt + 0, 1);
    } else {
      bar_lds();
      w_voff(lane, voff);
      int G = ((jj >> 3) * 4 + (xcd >> 1) + (w - 6) * 8) % 15;
#pragma unroll 1
      for (int i = w - 6; i < 8; ++i) {
        const char* sb = (const char*)wq + hg * 160 + (size_t)(5 + G) * 40960;
#pragma unroll
        for (int c = 0; c < 5; ++c) dma16_if(G * 5 + c != 74, sb, voff[c], lds0 + L_W + (5 + G) * 5120 + c * 1024);
        G = G == 14 ? 0 : G + 1;
      }
    }

    load_piece(xpiece, lane, xp);
    late = *(const u32x4*)((const char*)wq + hg * 160 + 19 * 40960 + voff[4]);
    bounce_piece(smem + L_W + (w - 4) * 5376, lane, xp, af);
#pragma unroll
    for (int s = 0; s < 10; ++s) asm volatile("" : "+v"(af[s]));
    asm volatile("" : "+v"(late));
    if (lane == 0) lds_add(cnt + 4, 1);

    if (w == 4) {
      lds_wait(cnt + 4, 4);
#pragma unroll
      for (int c = 0; c < 5; ++c) dma16((const char*)wq + hg * 160 + 4 * 40960, voff[c], lds0 + L_W + 4 * 5120 + c * 1024);
    }

  }
  float s1 = 0.f, s2q = 0.f;
#pragma unroll
  for (int s = 0; s < 10; ++s) {
    const u32x4 u = __builtin_bit_cast(u32x4, af[s]);
#pragma unroll
    for (int e = 0; e < 4; ++e) { s1 = dot2(u[e], 0x3f803f80u, s1); s2q = dot2(u[e], u[e], s2q); }
  }
  s1 = sum_halves(sum_rows(s1)); s2q = sum_halves(sum_rows(s2q));
  bar_all();

  if (lane < 16) *(LAS f32x2*)(smem + L_STAT + (kh * 64 + mt * 16 + lane) * 8) = f32x2{s1, s2q};
  KVRegs kv;
  const int k2o = (lane < 32 ? lane : 32) * 8, vo = (lane >> 5) * 352 + ((lane & 31) < 21 ? (lane & 31) : 21) * 16;
  auto load_tile = [&](int t) {
    kv.k1[t] = __builtin_bit_cast(bf16x8, ld16(rs, lane * 16, WS_KF1 + (w * 8 + t) * 1024));
    kv.k2[t] = __builtin_bit_cast(s16x4, ld8(rs, k2o, WS_KF2 + (w * 8 + t) * 272));
    kv.v[t][0] = __builtin_bit_cast(bf16x8, ld16(rs, vo, WS_VF + (w * 8 + t) * 1408));
    if (t < 7) kv.v[t][1] = __builtin_bit_cast(bf16x8, ld16(rs, vo + 704, WS_VF + (w * 8 + t) * 1408));
  };
  u32x4 gv[4];
  if (w == 0) {
#pragma unroll
    for (int i = 0; i < 4; ++i)
      asm volatile("global_load_dwordx4 %0, %1, %2 sc1" : "=v"(gv[i]) : "v"(lane * 128 + i * 8192), "s"(ws + WS_FB) : "memory");
  }
  bar_lds();

  if (kh == 1) *(LAS u32x4*)(smem + L_W + 99 * 1024 + lane * 16) = late;
  {
    f32x4 acc[5] = {{0, 0, 0, 0}, {0, 0, 0, 0}, {0, 0, 0, 0}, {0, 0, 0, 0}, {0, 0, 0, 0}};
    f32x4 cs[5] = {{0, 0, 0, 0}, {0, 0, 0, 0}, {0, 0, 0, 0}, {0, 0, 0, 0}, {0, 0, 0, 0}};
    const bf16x8 ones = __builtin_bit_cast(bf16x8, u32x4{0x3f803f80u, 0x3f803f80u, 0x3f803f80u, 0x3f803f80u});
    gemm_half(af, smem + L_W + kh * 51200, lane, acc, [&](int t, const bf16x8 (&wf)[5]) {
      if ((t & 3) == mt) {
#pragma unroll
        for (int i = 0; i < 5; ++i) cs[i] = MFMA16(wf[i], ones, cs[i]);
      }
    });
    write_part(smem + L_PART, kh, mt, lane, acc);
    if ((lane & 15) == 0) {
#pragma unroll
      for (int i = 0; i < 5; ++i) *(LAS f32x4*)(smem + L_CS + w * 320 + i * 64 + (lane >> 4) * 16) = cs[i];
    }
  }
  if (w == 0) {
    asm volatile("s_waitcnt vmcnt(0)" : "+v"(gv[0]), "+v"(gv[1]), "+v"(gv[2]), "+v"(gv[3]) :: "memory");
#pragma unroll 1
      for (unsigned spin = 0; spin < SPIN_CAP; ++spin) {
        bool ok = true;
#pragma unroll
        for (int i = 0; i < 4; ++i) ok &= (gv[i][0] == tag) & (gv[i][2] == tag);
        if (__all(ok)) break;
        if (spin) __builtin_amdgcn_s_sleep(POLL_SLEEP);
        CFENCE();
#pragma unroll
        for (int i = 0; i < 4; ++i)
          if ((gv[i][0] != tag) | (gv[i][2] != tag)) gv[i] = ld16(rs, lane * 128 + i * 8192, WS_FB);
      }
      CFENCE();
      float rm = __uint_as_float(gv[0][1]), kn = __uint_as_float(gv[0][3] & 0xffff0000u);
#pragma unroll
      for (int i = 1; i < 4; ++i) { rm = fmaxf(rm, __uint_as_float(gv[i][1])); kn = fmaxf(kn, __uint_as_float(gv[i][3] & 0xffff0000u)); }
      rm = max64(rm); kn = max64(kn);
      float neg = -4096.f;
#pragma unroll
      for (int i = 0; i < 4; ++i) neg = __uint_as_float(gv[i][1]) == rm ? fmaxf(neg, -(float)(gv[i][3] & 0xffffu)) : neg;
      neg = max64(neg);
      if (lane == 0) {
        *(LAS u32x4*)(smem + L_SUM) = u32x4{__float_as_uint(rm), (unsigned)(int)(-neg) & (S_ - 1), __float_as_uint(kn), tag};
        if (bid == 0) st4(rs, tag, WS_EP);
      }
    }
  bar_lds();

  load_tile(0);
  kv.v[7][1] = __builtin_bit_cast(bf16x8, ld16(rs, vo + 704, WS_VF + (w * 8 + 7) * 1408));
  load_tile(1);
  SB();
  const u32x4 sumv = *(const LAS u32x4*)(smem + L_SUM);
  const float rmax = __uint_as_float(__builtin_amdgcn_readfirstlane(sumv[0]));
  const int ridx = __builtin_amdgcn_readfirstlane(sumv[1]);
  tag = __builtin_amdgcn_readfirstlane(sumv[3]);
  unsigned xs[5] = {0, 0, 0, 0, 0};
  float qn2 = 0.f;
  if (tid < 256) {
    const int b = tid >> 5, cc = tid & 31, tok = lane;
    const char LAS* pp = smem + L_PART + tok * 336 + w * 80;
    f32x4 cq = *(const LAS f32x4*)(smem + L_CS + (lane & 7) * 320 + w * 80 + (lane < 40 ? lane >> 3 : 0) * 16);
    const f32x2 st = *(const LAS f32x2*)(smem + L_STAT + tok * 8) + *(const LAS f32x2*)(smem + L_STAT + 512 + tok * 8);
#pragma unroll
    for (int e = 0; e < 4; ++e) { cq[e] += dpp_mov<0xB1>(cq[e]); cq[e] += dpp_mov<0x4E>(cq[e]); cq[e] += dpp_mov<0x141>(cq[e]); }
    const float mean = st[0] * (1.0f / D_);
    const float sd = ln_sd(fmaxf(fmaf(-mean, mean, st[1] * (1.0f / D_)), 0.f));
    const float mb = rbf(mean), sc = QSCALE / sd;
    if (w == 0) *(LAS unsigned*)(smem + L_STP + tok * 4) = pk2(mb, sd);
    unsigned qv[10];
#pragma unroll
    for (int j = 0; j < 5; ++j) {
      f32x4 a = *(const LAS f32x4*)(pp + j * 16) + *(const LAS f32x4*)(pp + 21504 + j * 16);
#pragma unroll
      for (int e = 0; e < 4; ++e) a[e] = fmaf(-mb, rdlane(cq[e], j * 8), a[e]) * sc;
      qv[2 * j] = pk2(a[0], a[1]); qv[2 * j + 1] = pk2(a[2], a[3]);
      qn2 = dot2(qv[2 * j], qv[2 * j], qn2); qn2 = dot2(qv[2 * j + 1], qv[2 * j + 1], qn2);
    }
    char LAS* qd = smem + L_Q + b * 1536;
    *(LAS u32x4*)(qd + cc * 16) = u32x4{qv[0], qv[1], qv[2], qv[3]};
    *(LAS u32x4*)(qd + (cc + 32) * 16) = u32x4{qv[4], qv[5], qv[6], qv[7]};
    *(LAS u32x2*)(qd + 1024 + cc * 8) = u32x2{qv[8], qv[9]};
    const float m = __builtin_amdgcn_sqrtf(qn2 * __uint_as_float(sumv[2])) * 1.01f;
    *(LAS u32x2*)(qd + 1024 + (cc + 32) * 8) = u32x2{pk2(-m, 0.f), 0u};
  } else {
#pragma unroll
    for (int i = 0; i < 12; ++i) *(LAS f32x4*)(smem + L_RING + i * 4096 + (tid - 256) * 16) = f32x4{0, 0, 0, 0};
  }
  bar_lds();

  load_tile(2);
  load_tile(3);
  SB();
  const unsigned stp0 = *(const LAS unsigned*)(smem + L_STP + (tid / 10) * 4);
  const unsigned stp1 = *(const LAS unsigned*)(smem + L_STP + (tid < 128 ? (512 + tid) / 10 : 63) * 4);

  const f32x16 zero16 = {0, 0, 0, 0, 0, 0, 0, 0, 0, 0, 0, 0, 0, 0, 0, 0};
  f32x16 oa = zero16, ob = zero16;
  bf16x8 q1 = *(const LAS bf16x8*)(smem + L_Q + w * 1536 + lane * 16);
  s16x4 q2 = *(const LAS s16x4*)(smem + L_Q + w * 1536 + 1024 + lane * 8);
  f32x16 sa = MFMA32(kv.k1[0], q1, zero16), sb;
  sa = MFMA32K8(kv.k2[0], q2, sa);
  u32x4 p1 = {0, 0, 0, 0};
  att_block(kv, smem, lane, ((w + 7) & 7) + 8, ((w + 1) & 7) + 8, (w + 1) & 7, q1, q2, sa, sb, p1, ob, oa,
            [&](int i) {
              if (i < 4) load_tile(4 + i);
              if (i == 2 && w == 6) {
#pragma unroll
                for (int k = 0; k < 5; ++k)
                  asm volatile("global_load_dword %0, %1, %2 offset:%3" : "=v"(xs[k]) : "v"(lane * 4), "s"(x + (size_t)ridx * D_), "i"(k * 256) : "memory");
              }
            });
  if (w == 6) {
    asm volatile("s_waitcnt vmcnt(0)" : "+v"(xs[0]), "+v"(xs[1]), "+v"(xs[2]), "+v"(xs[3]), "+v"(xs[4]) :: "memory");
    float s = 0.f;
#pragma unroll
    for (int i = 0; i < 5; ++i) s = dot2(xs[i], 0x3f803f80u, s);
    const float mean = sum_halves(sum32(s)) * (1.0f / D_);
    f32x2 v2 = {0.f, 0.f};
#pragma unroll
    for (int i = 0; i < 5; ++i) { const f32x2 d = f32x2{bflo(xs[i]), bfhi(xs[i])} - mean; v2 += d * d; }
    const float sd = ln_sd(sum_halves(sum32(v2[0] + v2[1])) * (1.0f / D_));
    const float mb = rbf(mean), rc = 1.0f / sd;
#pragma unroll
    for (int i = 0; i < 5; ++i) *(LAS unsigned*)(smem + L_XS + i * 256 + lane * 4) = ln_pair(xs[i], mb, sd, rc);
  }
  DmaJob dj;
  if (w < 6) {
    const int ln = opaque(lane);
    const unsigned vo2 = w < 4 ? ln * (F_ * 2) : ln * 16;
#pragma unroll
    for (int i = 0; i < 5; ++i) dj.voff[i] = vo2;
    dj.sstr = w < 4 ? 64 * F_ * 2 : 1024;
    dj.sb = (w < 4 ? (const char*)((w < 2 ? wg : wu) + fcol) : (const char*)(wd + (size_t)fcol * D_)) + (size_t)(w & 1) * 5 * dj.sstr;
    dj.lds = lds0 + L_G + w * 5120;
    dj.on = has_ffn;
  } else {
    const int ln = opaque(lane);
    const char* qbase = (const char*)wo + hg * 160 + (size_t)(jj >> 3) * 204800 + (size_t)(w - 6) * 81920;
    touch4(qbase, (ln >> 1) * 1280 + (ln & 1) * 128, lds0 + L_FRED + 512);
    touch4(qbase + 40960, (ln >> 1) * 1280 + (ln & 1) * 128, lds0 + L_FRED + 512);
    if (w == 7) touch4(qbase + 81920, (ln >> 1) * 1280 + (ln & 1) * 128, lds0 + L_FRED + 512);
#pragma unroll
    for (int i = 0; i < 5; ++i) dj.voff[i] = 0;
    dj.sstr = 0; dj.sb = (const char*)wo; dj.lds = lds0; dj.on = 0;
  }
  u32x4 xe[2], ge[2];
  f32x4 yl[3];
  const bool sel_blk = (ridx >> 6) == qb;
#pragma unroll 1
  for (int attempt = 0;; ++attempt) {
#pragma unroll 1
    for (int st = 0; st < 8; st += 2) {
      int flag_off = -1;
      if (attempt == 0) {
        if (st == 4) {
          if (has_ffn && w < 4) {
            const int ln = opaque(lane);
            const int g = ln >> 4, q = (ln & 15) >> 2, pp = ln & 3;
            const char LAS* gb = smem + L_G + (pp >> 1) * 10240 + (8 * g + q) * 16 + (pp & 1) * 8 + w * 2560;
            const char LAS* xsp = smem + L_XS + w * 320 + g * 16;
            f32x4 a = {0, 0, 0, 0};
#pragma unroll
            for (int s = 0; s < 5; ++s)
              a = MFMA16(*(const LAS bf16x8*)(xsp + s * 64), cat8(trread(gb + s * 512), trread(gb + s * 512 + 64)), a);
            if (ln < 16) *(LAS float*)(smem + L_FRED + ln * 16 + w * 4) = a[0];
          }
        } else if (st == 6) {
          drain();
          if (has_ffn && w == 2) flag_off = WS_FY + pid * 128;
        }
      }
      if (attempt != 0 || st != 0)
        att_block(kv, smem, lane, ((w + st + 7) & 7) + 8, ((w + st + 1) & 7) + 8, (w + st + 1) & 7, q1, q2, sa, sb, p1, ob, oa,
                  [&](int i) { if (i == 5 && flag_off >= 0 && lane == 0) st4(rs, tag, flag_off); });
      unsigned waitmode = 0, yflag = 0;
      bool ylook = false;
      if (attempt == 0) {
        if (st == 2) {
          waitmode = 1;
          w_voff(opaque(lane), dj.voff);
          dj.sstr = 0;
          dj.sb = (const char*)wo + hg * 160 + (size_t)w * 40960; dj.lds = lds0 + L_W + w * 5120; dj.on = 1;
        } else if (st == 4) {
          dj.sb = (const char*)wo + hg * 160 + (size_t)(w < 2 ? 8 + w : 15 + w) * 40960;
          dj.lds = lds0 + (w < 2 ? L_W + (8 + w) * 5120 : L_WHI + (5 + w) * 5120); dj.on = w < 4;
          if (has_ffn && w >= 4) {
            const int ln = opaque(lane);
            const int cl = ln < 40 ? ln : 39;
            const f32x4 g4 = *(const LAS f32x4*)(smem + L_FRED + (ln & 15) * 16);
            u32x2 wv2[8];
#pragma unroll
            for (int c = 0; c < 8; ++c) wv2[c] = *(const LAS u32x2*)(smem + L_DN + c * 1280 + (w - 4) * 320 + cl * 8);
            const float gu = rbf((g4[0] + g4[1]) + (g4[2] + g4[3]));
            const float up = dpp_mov<0x128>(gu);
            const float hval = rbf(rbf(silu_f(gu)) * up);
            f32x4 y = {0, 0, 0, 0};
#pragma unroll
            for (int c = 0; c < 8; ++c) {
              const float hc = rdlane(hval, c);
              y[0] = fmaf(hc, bflo(wv2[c][0]), y[0]); y[1] = fmaf(hc, bfhi(wv2[c][0]), y[1]);
              y[2] = fmaf(hc, bflo(wv2[c][1]), y[2]); y[3] = fmaf(hc, bfhi(wv2[c][1]), y[3]);
            }
            if (ln < 40) st16(rs, __builtin_bit_cast(u32x4, y), ln * 16 + (ln >= 20 ? 2560 - 320 : 0),
                              WS_YP + (qb * 8 + (w - 4) * 2) * 2560 + hg * 320);
          }
        } else if (st == 6) {
          dj.sb = (const char*)wo + hg * 160 + (size_t)(w < 7 ? 10 + w : 19) * 40960;
          dj.lds = lds0 + L_WHI + (w < 7 ? w : 9) * 5120; dj.on = 1;
          if (has_ffn && w == 3) {
            asm volatile("global_load_dword %0, %1, %2 sc1" : "=v"(yflag) : "v"((lane & 7) * 128), "s"(ws + WS_FY + qb * 1024) : "memory");
            ylook = true;
          }
        }
      }
      att_block(kv, smem, lane, (w + st) & 7, (w + st + (st == 6 ? 1 : 2)) & 7, (w + st + 2) & 7, q1, q2, sa, sb, p1, oa, ob,
                [&](int i) {
                  dma_hook(dj, waitmode, i);
                  if (i == 3 && ylook) {
                    asm volatile("s_waitcnt vmcnt(0)" : "+v"(yflag) :: "memory");
                    if (!__all(yflag == tag)) poll_words(rs, (lane & 7) * 128, WS_FY + qb * 1024, tag);
                    CFENCE();
                    const int ln = opaque(lane);
#pragma unroll
                    for (int k = 0; k < 3; ++k)
                      yl[k] = __builtin_bit_cast(f32x4, ld16(rs, (ln < 60 && k * 60 + ln < 160) ? k * 960 + ln * 16 : 0x7ff00000, WS_YP + pid * 2560));
                  }
                });
      dj.on = 0;

    }
    __builtin_amdgcn_s_setprio(0);
    ob = MFMA32(kv.v[7][1], __builtin_bit_cast(bf16x8, p1), ob);
#pragma unroll
    for (int e = 0; e < 12; ++e) ob[e] += oa[e];

    const int tid4 = opaque(tid), ln4 = tid4 & 63;
    const float l = hi_half(ob[8]);
    const bool bad = attempt == 0 && __any(!(l >= 5.4e-20f && l <= 1e30f));
    LAS unsigned* ocnt = (LAS unsigned*)(smem + L_CNT);
    if (!bad) {
      const int b = (w + 7) & 7, hh = b >> 1, tok = qb * 64 + (b & 1) * 32 + (ln4 & 31), h4 = ln4 >> 5;
      const float inv = 1.0f / l;
      unsigned A0 = pk2(ob[0] * inv, ob[1] * inv), A1 = pk2(ob[2] * inv, ob[3] * inv);
      unsigned B0 = pk2(ob[4] * inv, ob[5] * inv), B1 = pk2(ob[6] * inv, ob[7] * inv);
      unsigned C0 = pk2(ob[8] * inv, ob[9] * inv), C1 = pk2(ob[10] * inv, ob[11] * inv);
      const int base = WS_O + ((tok >> 4) * 80 + hg * 10 + ((hh * 5) >> 1)) * 256 + (tok & 15) * 16;
      if ((hh & 1) == 0) {
        auto s0 = __builtin_amdgcn_permlane32_swap(A0, B0, false, false);
        auto s1 = __builtin_amdgcn_permlane32_swap(A1, B1, false, false);
        st16(rs, u32x4{s0[0], s1[0], s0[1], s1[1]}, base + h4 * 256);
        if (h4 == 0) st8(rs, u32x2{C0, C1}, base + 512);
      } else {
        auto s0 = __builtin_amdgcn_permlane32_swap(A0, C0, false, false);
        auto s1 = __builtin_amdgcn_permlane32_swap(A1, C1, false, false);
        st16(rs, h4 == 0 ? u32x4{s0[1], s1[1], B0, B1} : u32x4{B0, B1, s0[0], s1[0]}, base + 256 + h4 * 256);
        if (h4 == 0) st8(rs, u32x2{s0[0], s1[0]}, base + 8);
      }
    }
    drain();
    if (ln4 == 0 && lds_add_rtn(ocnt, bad ? 0x101u : 1u) == 7u && !bad) st4(rs, tag, WS_FC + pid * 128);
    if (attempt == 0 && has_ffn && w == 3) {
      f32x4 a = (yl[0] + yl[1]) + yl[2];
#pragma unroll
      for (int e = 0; e < 4; ++e) a[e] = (a[e] + __shfl_down(a[e], 20)) + __shfl_down(a[e], 40);
      if (ln4 < 20) st16(rs, __builtin_bit_cast(u32x4, a), ln4 * 16, WS_YQ + (hg * 27 + qb) * 320);
      drain();
      if (ln4 == 0) st4(rs, tag, WS_FZ + hg * 4096 + qb * 128);
    }

#pragma unroll
    for (int i = 0; i < 2; ++i) {
      if (i == 1 && w >= 2) { xe[1] = u32x4{0, 0, 0, 0}; break; }
      const int n = i * 512 + tid4;
      const int row = n / 10, ch = n - row * 10, tok = qb * 64 + row;
      xe[i] = *(const u32x4*)(x + (size_t)tok * D_ + hg * 80 + ch * 8);
    }
    const bool selrow0 = qb * 64 + tid4 / 10 == ridx, selrow1 = qb * 64 + (512 + tid4) / 10 == ridx;
#pragma unroll
    for (int i = 0; i < 2; ++i) {
      if (i == 1 && w >= 2) { ge[1] = u32x4{0, 0, 0, 0}; break; }
      const unsigned stp = i == 0 ? stp0 : stp1;
      const float mb = bflo(stp), sd = bfhi(stp), rc = 1.0f / sd;
#pragma unroll
      for (int e = 0; e < 4; ++e) ge[i][e] = ln_pair(xe[i][e], mb, sd, rc);
      if (!(i == 0 ? selrow0 : selrow1)) {
#pragma unroll
        for (int e = 0; e < 4; ++e) ge[i][e] = pk2(silu_f(bflo(ge[i][e])), silu_f(bfhi(ge[i][e])));
      }
    }
    if (w == 7) {
#pragma unroll 1
      for (unsigned spin = 0; spin < SPIN_CAP; ++spin) {
        CFENCE();
        const unsigned v = ld4(rs, (ln4 & 7) * 128, WS_FC + qb * 1024);
        if (__all(v == tag) || lds_get(ocnt) > 0xffu) break;
        __builtin_amdgcn_s_sleep(1);
      }
    }
    bar_all();
    if (lds_get(ocnt) <= 0xffu) break;
    {
      const int c32 = ln4 & 31, h = ln4 >> 5;
#pragma unroll 1
      for (int b = 0; b < 8; ++b) {
        const bf16x8 fq1 = *(const LAS bf16x8*)(smem + L_Q + b * 1536 + ln4 * 16);
        s16x4 fq2 = *(const LAS s16x4*)(smem + L_Q + b * 1536 + 1024 + ln4 * 8);
        if (h == 1) fq2 = s16x4{0, 0, 0, 0};
        float mx = -INFINITY;
#pragma unroll
        for (int t = 0; t < 8; ++t) {
          f32x16 s = MFMA32(kv.k1[t], fq1, zero16);
          s = MFMA32K8(kv.k2[t], fq2, s);
#pragma unroll
          for (int e = 0; e < 16; ++e) mx = fmaxf(mx, s[e]);
        }
        mx = fmaxf(mx, __shfl_xor(mx, 32));
        if (h == 0) *(LAS float*)(smem + L_MAXS + (w * 256 + b * 32 + c32) * 4) = mx;
      }
      bar_lds();
      if (tid4 < 256) {
        float mx = -INFINITY;
#pragma unroll
        for (int i = 0; i < 8; ++i) mx = fmaxf(mx, *(LAS float*)(smem + L_MAXS + (i * 256 + tid4) * 4));
        const float hi = rbf(mx), lo = mx - hi;
        *(LAS u32x2*)(smem + L_Q + (tid4 >> 5) * 1536 + 1024 + ((tid4 & 31) + 32) * 8) = u32x2{pk2(-hi, -lo), 0u};
      }
      bar_lds();
#pragma unroll
      for (int i = 0; i < 6; ++i) *(LAS f32x4*)(smem + L_RING + i * 8192 + tid4 * 16) = f32x4{0, 0, 0, 0};
      bar_lds();
      q1 = *(const LAS bf16x8*)(smem + L_Q + w * 1536 + lane * 16);
      q2 = *(const LAS s16x4*)(smem + L_Q + w * 1536 + 1024 + lane * 8);
      sa = MFMA32(kv.k1[0], q1, zero16);
      sa = MFMA32K8(kv.k2[0], q2, sa);
      p1 = u32x4{0, 0, 0, 0};
      oa = zero16; ob = zero16;
    }
  }

  const int tid5 = opaque(tid), ln5 = tid5 & 63;
#pragma unroll
  for (int s = 0; s < 5; ++s)
    af[s] = __builtin_bit_cast(bf16x8, ld16(rs, ln5 * 16 + s * 1024, WS_O + ((qb * 4 + mt) * 20 + kh * 10) * 1024));
  LAS float* ys = (LAS float*)(smem + L_YS);
  {
    f32x4 acc[5] = {{0, 0, 0, 0}, {0, 0, 0, 0}, {0, 0, 0, 0}, {0, 0, 0, 0}, {0, 0, 0, 0}};
    gemm_half(af, smem + (kh ? L_WHI : L_W), ln5, acc, [&](int t, const bf16x8 (&)[5]) {
      if (t < 5) af[t + 5] = __builtin_bit_cast(bf16x8, ld16(rs, ln5 * 16 + (t + 5) * 1024, WS_O + ((qb * 4 + mt) * 20 + kh * 10) * 1024));
    });
    write_part(smem + L_PART2, kh, mt, ln5, acc);
  }

  f32x4 yt[9];
  if (sel_blk && w == 0) {
    poll_words(rs, (ln5 < 27 ? ln5 : 26) * 128, WS_FZ + hg * 4096, tag);
#pragma unroll
    for (int i = 0; i < 9; ++i) yt[i] = __builtin_bit_cast(f32x4, ld16(rs, (ln5 < 60 ? ln5 : 59) * 16 + i * 960, WS_YQ + hg * 8640));
  }
  bar_lds();
  auto finish = [&](int i, bool sel_pass) {
    const int n = i * 512 + tid5;
    const int row = n / 10, ch = n - row * 10, tok = qb * 64 + row;
    if (n < 640 && (tok == ridx) == sel_pass) {
      const char LAS* pp = smem + L_PART2 + row * 336 + ch * 32;
      const f32x4 s0 = *(const LAS f32x4*)(pp) + *(const LAS f32x4*)(pp + 21504);
      const f32x4 s1 = *(const LAS f32x4*)(pp + 16) + *(const LAS f32x4*)(pp + 21504 + 16);
      if (sel_pass) {
#pragma unroll
        for (int e = 0; e < 4; ++e)
          ge[i][e] = pk2(silu_f(rbf(bflo(ge[i][e]) + ys[ch * 8 + 2 * e])), silu_f(rbf(bfhi(ge[i][e]) + ys[ch * 8 + 2 * e + 1])));
      }
      u32x4 r;
      r[0] = pk2(bflo(xe[i][0]) + rbf(rbf(s0[0]) * bflo(ge[i][0])), bfhi(xe[i][0]) + rbf(rbf(s0[1]) * bfhi(ge[i][0])));
      r[1] = pk2(bflo(xe[i][1]) + rbf(rbf(s0[2]) * bflo(ge[i][1])), bfhi(xe[i][1]) + rbf(rbf(s0[3]) * bfhi(ge[i][1])));
      r[2] = pk2(bflo(xe[i][2]) + rbf(rbf(s1[0]) * bflo(ge[i][2])), bfhi(xe[i][2]) + rbf(rbf(s1[1]) * bfhi(ge[i][2])));
      r[3] = pk2(bflo(xe[i][3]) + rbf(rbf(s1[2]) * bflo(ge[i][3])), bfhi(xe[i][3]) + rbf(rbf(s1[3]) * bfhi(ge[i][3])));
      *(u32x4*)(out + (size_t)tok * D_ + hg * 80 + ch * 8) = r;
    }
  };
#pragma unroll 1
  for (int pass = 0; pass < 2; ++pass) {
    finish(0, pass == 1);
    finish(1, pass == 1);
    if (!sel_blk || pass == 1) break;
    if (w == 0) {
      f32x4 a = yt[0];
#pragma unroll
      for (int i = 1; i < 9; ++i) a += yt[i];
      const float rsig = rbf(sigm_f(rbf(rmax)));
#pragma unroll
      for (int e = 0; e < 4; ++e) a[e] = rbf(rbf((a[e] + __shfl_down(a[e], 20)) + __shfl_down(a[e], 40)) * rsig);
      if (ln5 < 20) *(LAS f32x4*)(ys + ln5 * 4) = a;
    }
    bar_lds();
  }

}

extern "C" void kernel_launch(void* const* d_in, const int* in_sizes, int n_in, void* d_out, int out_size,
                              void* d_ws, size_t ws_size, hipStream_t stream) {
  fused<<<256, 512, 0, stream>>>((const u16*)d_in[0], (const u16*)d_in[1], (const u16*)d_in[2], (const u16*)d_in[3],
                                 (const u16*)d_in[4], (const u16*)d_in[6], (const u16*)d_in[7], (const u16*)d_in[8],
                                 (const u16*)d_in[9], (char*)d_ws, (u16*)d_out);
}
```

```cpp
#include <hip/hip_runtime.h>
#include <hip/hip_bf16.h>
#include <stdint.h>

typedef __attribute__((ext_vector_type(8))) __bf16 bf16x8;
typedef __attribute__((ext_vector_type(2))) __bf16 bf16x2;
typedef __attribute__((ext_vector_type(4))) short s16x4;
typedef __attribute__((ext_vector_type(2))) float f32x2;
typedef __attribute__((ext_vector_type(4))) float f32x4;
typedef __attribute__((ext_vector_type(16))) float f32x16;
typedef __attribute__((ext_vector_type(2))) unsigned u32x2;
typedef __attribute__((ext_vector_type(4))) unsigned u32x4;
typedef unsigned short u16;
#define LAS __attribute__((address_space(3)))
#define DI __device__ __forceinline__
#define SB() __builtin_amdgcn_sched_barrier(0)
#define CFENCE() asm volatile("" ::: "memory")

constexpr int S_ = 2048, D_ = 640, F_ = 1728;

constexpr int WS_O = 0;
constexpr int WS_KF1 = WS_O + S_ * D_ * 2;
constexpr int WS_KF2 = WS_KF1 + 65536;
constexpr int WS_VF = WS_KF2 + 64 * 272;
constexpr int WS_YP = WS_VF + 64 * 1408;
constexpr int WS_YQ = WS_YP + 216 * D_ * 4;
constexpr int WS_FC = WS_YQ + 216 * 320 + 128 * 4;
constexpr int WS_FB = WS_FC + 32768;
constexpr int WS_FY = WS_FB + 32768;
constexpr int WS_FZ = WS_FY + 32768;
constexpr int WS_EP = WS_FZ + 32768;
static_assert(WS_FC % 128 == 0, "flag lines");
constexpr int WS_BYTES = WS_EP + 256;

constexpr int L_W = 0;
constexpr int L_SCR = 101376;
constexpr int L_WK = 102400, L_WV = 128000, L_X = 153600;
constexpr int L_WHI = 112640;
constexpr int L_RING = 51200;
constexpr int L_Q = 100352;
constexpr int L_PART = 112640;
constexpr int L_G = 112640, L_U = 122880, L_DN = 133120;
constexpr int L_FRED = 143360;
constexpr int L_XS = 161280;
constexpr int L_SUM = 162560;
constexpr int L_STAT = 155648;
constexpr int L_CS = 156672;
constexpr int L_STP = 159232;
constexpr int L_MAXS = L_RING;
constexpr int L_PART2 = L_RING;
constexpr int L_YS = L_RING + 48768;
constexpr int L_CNT = L_Q + 1024 + 256 + 4;
constexpr int LDS_BYTES = 163840;

constexpr float QSCALE = 0.22360679774997896f * 1.4426950408889634f;
constexpr unsigned SPIN_CAP = 1u << 18;
#define POLL_SLEEP 4

DI float bflo(unsigned v) { return __uint_as_float(v << 16); }
DI float bfhi(unsigned v) { return __uint_as_float(v & 0xffff0000u); }
DI unsigned pk2(float a, float b) {
  f32x2 t = {a, b};
  return __builtin_bit_cast(unsigned, __builtin_convertvector(t, bf16x2));
}
DI float rbf(float a) { return bflo(pk2(a, a)); }
DI float dot2(unsigned a, unsigned b, float c) {
  return __builtin_amdgcn_fdot2_f32_bf16(__builtin_bit_cast(bf16x2, a), __builtin_bit_cast(bf16x2, b), c, false);
}
DI s16x4 trread(const char LAS* p) {
  return __builtin_bit_cast(s16x4, __builtin_amdgcn_ds_read_tr16_b64_v4i16((LAS s16x4*)p));
}
DI bf16x8 cat8(s16x4 a, s16x4 b) {
  return __builtin_bit_cast(bf16x8, __builtin_shufflevector(a, b, 0, 1, 2, 3, 4, 5, 6, 7));
}
#define MFMA16(a, b, c) __builtin_amdgcn_mfma_f32_16x16x32_bf16((a), (b), (c), 0, 0, 0)
#define MFMA32(a, b, c) __builtin_amdgcn_mfma_f32_32x32x16_bf16((a), (b), (c), 0, 0, 0)
#define MFMA32K8(a, b, c) __builtin_amdgcn_mfma_f32_32x32x8bf16_1k((a), (b), (c), 0, 0, 0)
#define EXP2(x) __builtin_amdgcn_exp2f(x)
DI float sigm_f(float v) { return __builtin_amdgcn_rcpf(1.0f + EXP2(v * -1.4426950408889634f)); }
DI float silu_f(float v) { return v * sigm_f(v); }
DI void bar_lds() { asm volatile("s_waitcnt lgkmcnt(0)\n\ts_barrier" ::: "memory"); }
DI void bar_all() { asm volatile("s_waitcnt vmcnt(0) lgkmcnt(0)\n\ts_barrier" ::: "memory"); }
DI void drain() { asm volatile("s_waitcnt vmcnt(0)" ::: "memory"); }
DI void lds_add(LAS unsigned* p, unsigned v) {
  asm volatile("s_waitcnt lgkmcnt(0)" ::: "memory");
  __hip_atomic_fetch_add(p, v, __ATOMIC_RELAXED, __HIP_MEMORY_SCOPE_WORKGROUP);
}
DI unsigned lds_add_rtn(LAS unsigned* p, unsigned v) {
  asm volatile("s_waitcnt lgkmcnt(0)" ::: "memory");
  const unsigned r = __hip_atomic_fetch_add(p, v, __ATOMIC_RELAXED, __HIP_MEMORY_SCOPE_WORKGROUP);
  CFENCE();
  return r;
}
DI unsigned lds_get(LAS unsigned* p) {
  CFENCE();
  const unsigned r = __builtin_amdgcn_readfirstlane(__hip_atomic_load(p, __ATOMIC_RELAXED, __HIP_MEMORY_SCOPE_WORKGROUP));
  CFENCE();
  return r;
}
DI void lds_wait(LAS unsigned* p, unsigned target) {
#pragma unroll 1
  while (lds_get(p) < target) __builtin_amdgcn_s_sleep(1);
}
DI void dma16(const void* sbase, unsigned voff, unsigned lds) {
  unsigned keep;
  asm volatile("s_mov_b32 %0, m0\n\ts_mov_b32 m0, %3\n\ts_nop 3\n\tglobal_load_lds_dwordx4 %1, %2\n\ts_mov_b32 m0, %0"
               : "=&s"(keep) : "v"(voff), "s"(sbase), "s"(lds) : "memory");
}
DI void dma16_if(unsigned on, const void* sbase, unsigned voff, unsigned lds) {
  unsigned keep;
  on = __builtin_amdgcn_readfirstlane(on);
  lds = __builtin_amdgcn_readfirstlane(lds);
  sbase = (const void*)(((unsigned long long)__builtin_amdgcn_readfirstlane((unsigned)((unsigned long long)sbase >> 32)) << 32) |
                        (unsigned)__builtin_amdgcn_readfirstlane((unsigned)(unsigned long long)sbase));
  asm volatile("s_cmp_eq_u32 %4, 0\n\ts_cbranch_scc1 1f\n\ts_mov_b32 %0, m0\n\ts_mov_b32 m0, %3\n\ts_nop 1\n\t"
               "global_load_lds_dwordx4 %1, %2\n\ts_mov_b32 m0, %0\n1:"
               : "=&s"(keep) : "v"(voff), "s"(sbase), "s"(lds), "s"(on) : "memory", "scc");
}
DI void touch4(const void* sbase, unsigned voff, unsigned lds) {
  unsigned keep;
  asm volatile("s_mov_b32 %0, m0\n\ts_mov_b32 m0, %3\n\ts_nop 3\n\tglobal_load_lds_dword %1, %2\n\ts_mov_b32 m0, %0"
               : "=&s"(keep) : "v"(voff), "s"(sbase), "s"(lds) : "memory");
}
typedef __amdgpu_buffer_rsrc_t rsrc_t;
DI u32x4 ld16(rsrc_t r, int v, int s = 0) { return __builtin_amdgcn_raw_buffer_load_b128(r, v, s, 16); }
DI u32x2 ld8(rsrc_t r, int v, int s = 0) { return __builtin_amdgcn_raw_buffer_load_b64(r, v, s, 16); }
DI unsigned ld4(rsrc_t r, int v, int s = 0) { return __builtin_amdgcn_raw_buffer_load_b32(r, v, s, 16); }
DI void st16(rsrc_t r, u32x4 d, int v, int s = 0) { __builtin_amdgcn_raw_buffer_store_b128(d, r, v, s, 16); }
DI void st8(rsrc_t r, u32x2 d, int v, int s = 0) { __builtin_amdgcn_raw_buffer_store_b64(d, r, v, s, 16); }
DI void st4(rsrc_t r, unsigned d, int v, int s = 0) { __builtin_amdgcn_raw_buffer_store_b32(d, r, v, s, 16); }

template <int CTRL> DI float dpp_mov(float v) {
  return __builtin_bit_cast(float, __builtin_amdgcn_update_dpp(0, __builtin_bit_cast(int, v), CTRL, 0xf, 0xf, true));
}
DI float sum16(float v) {
  v += dpp_mov<0xB1>(v); v += dpp_mov<0x4E>(v); v += dpp_mov<0x141>(v); v += dpp_mov<0x140>(v);
  return v;
}
DI float sum_rows(float v) {
  const unsigned b = __float_as_uint(v);
  auto r = __builtin_amdgcn_permlane16_swap(b, b, false, false);
  return __uint_as_float(r[0]) + __uint_as_float(r[1]);
}
DI float sum_halves(float v) {
  const unsigned b = __float_as_uint(v);
  auto r = __builtin_amdgcn_permlane32_swap(b, b, false, false);
  return __uint_as_float(r[0]) + __uint_as_float(r[1]);
}
DI float hi_half(float v) {
  const unsigned b = __float_as_uint(v);
  auto r = __builtin_amdgcn_permlane32_swap(b, b, false, false);
  return __uint_as_float(r[1]);
}
DI float max64(float v) {
  v = fmaxf(v, dpp_mov<0xB1>(v)); v = fmaxf(v, dpp_mov<0x4E>(v)); v = fmaxf(v, dpp_mov<0x141>(v)); v = fmaxf(v, dpp_mov<0x140>(v));
  auto r = __builtin_amdgcn_permlane16_swap(__float_as_uint(v), __float_as_uint(v), false, false);
  v = fmaxf(__uint_as_float(r[0]), __uint_as_float(r[1]));
  auto q = __builtin_amdgcn_permlane32_swap(__float_as_uint(v), __float_as_uint(v), false, false);
  return fmaxf(__uint_as_float(q[0]), __uint_as_float(q[1]));
}
DI int opaque(int v) { asm volatile("" : "+v"(v)); return v; }
DI float sum32(float v) { return sum_rows(sum16(v)); }
DI float rdlane(float v, int l) { return __builtin_bit_cast(float, __builtin_amdgcn_readlane(__builtin_bit_cast(int, v), l)); }

DI float ln_sd(float var) { return rbf(sqrtf(rbf(rbf(var) + 1e-5f))); }
DI unsigned ln_pair(unsigned xp, float mb, float sd, float rc) {
  const unsigned dp = pk2(bflo(xp) - mb, bfhi(xp) - mb);
  const f32x2 d = {bflo(dp), bfhi(dp)};
  const f32x2 q0 = d * rc;
  const f32x2 a = __builtin_elementwise_fma(__builtin_elementwise_fma(-q0, f32x2{sd, sd}, d), f32x2{rc, rc}, q0);
  return pk2(a[0], a[1]);
}
DI void load_piece(const u16* __restrict__ xrow0, int lane, u32x4 (&xp)[10]) {
#pragma unroll
  for (int i = 0; i < 5; ++i) {
    const int n = i * 64 + lane, row = n / 20, ch = n - row * 20;
    xp[i] = *(const u32x4*)((const char*)xrow0 + row * 1280 + ch * 16);
    xp[5 + i] = *(const u32x4*)((const char*)xrow0 + row * 1280 + ch * 16 + 320);
  }
}
DI void bounce_piece(char LAS* blk, int lane, const u32x4 (&xp)[10], bf16x8 (&af)[10]) {
#pragma unroll
  for (int hf = 0; hf < 2; ++hf) {
#pragma unroll
    for (int i = 0; i < 5; ++i) {
      const int n = i * 64 + lane, row = n / 20, ch = n - row * 20;
      *(LAS u32x4*)(blk + row * 336 + ch * 16) = xp[hf * 5 + i];
    }
#pragma unroll
    for (int s = 0; s < 5; ++s) af[hf * 5 + s] = *(const LAS bf16x8*)(blk + (lane & 15) * 336 + (lane >> 4) * 16 + s * 64);
  }
}
DI void w_voff(int lane, unsigned (&voff)[5]) {
#pragma unroll
  for (int i = 0; i < 5; ++i) {
    const int n = i * 64 + lane, p = n / 10, ch = n - p * 10;
    const int k = (p & ~12) | ((p & 4) << 1) | ((p & 8) >> 1);
    voff[i] = k * 1280 + ch * 16;
  }
}
template <class F>
DI void gemm_half(bf16x8 (&af)[10], const char LAS* wimg, int lane, f32x4 (&acc)[5], F side) {
  const int g = lane >> 4, q = (lane & 15) >> 2, pp = lane & 3;
  const char LAS* wb = wimg + (16 * (g >> 1) + 4 * (g & 1) + q) * 160 + 8 * pp;
  bf16x8 wf[3][5];
#pragma unroll
  for (int p = 0; p < 2; ++p)
#pragma unroll
    for (int i = 0; i < 5; ++i) wf[p][i] = cat8(trread(wb + p * 5120 + i * 32), trread(wb + p * 5120 + 1280 + i * 32));
#pragma unroll
  for (int s = 0; s < 10; ++s) {
#pragma unroll
    for (int i = 0; i < 5; ++i) {
      acc[i] = MFMA16(wf[s % 3][i], af[s], acc[i]);
      SB();
      if (s < 8) wf[(s + 2) % 3][i] = cat8(trread(wb + (s + 2) * 5120 + i * 32), trread(wb + (s + 2) * 5120 + 1280 + i * 32));
      SB();
    }
    side(s, wf[s % 3]);
    SB();
  }
}
DI void write_part(char LAS* part, int kh, int mt, int lane, const f32x4 (&acc)[5]) {
  char LAS* p = part + kh * 21504 + (mt * 16 + (lane & 15)) * 336 + (lane >> 4) * 16;
#pragma unroll
  for (int i = 0; i < 5; ++i) *(LAS f32x4*)(p + i * 64) = acc[i];
}

struct KVRegs { bf16x8 k1[8]; s16x4 k2[8]; bf16x8 v[8][2]; };
struct DmaJob { const char* sb; unsigned sstr, lds, on; unsigned voff[5]; };

#define EXP_PK(dst, S, b) { dst[0] = pk2(EXP2(S[b]), EXP2(S[b + 1])); dst[1] = pk2(EXP2(S[b + 2]), EXP2(S[b + 3])); }
template <bool NEWBLK, int PRIO>
DI void att_step(f32x16& sc, f32x16& sn, const bf16x8& k1n, const s16x4& k2n, const bf16x8& q1n, const s16x4& q2n,
                 const bf16x8& v0, const bf16x8& v1prev, u32x4& p1, f32x16& oprev, f32x16& ocur) {
  const f32x16 zero16 = {0, 0, 0, 0, 0, 0, 0, 0, 0, 0, 0, 0, 0, 0, 0, 0};
  u32x2 a, b, c, d;
  if (PRIO >= 0) __builtin_amdgcn_s_setprio(PRIO);
  sn = MFMA32(k1n, q1n, zero16); SB();
  EXP_PK(a, sc, 0); SB();
  sn = MFMA32K8(k2n, q2n, sn); SB();
  EXP_PK(b, sc, 4); SB();
  oprev = MFMA32(v1prev, __builtin_bit_cast(bf16x8, p1), oprev); SB();
  EXP_PK(c, sc, 8); SB();
  const u32x4 p0 = {a[0], a[1], b[0], b[1]};
  if (NEWBLK) ocur = MFMA32(v0, __builtin_bit_cast(bf16x8, p0), ocur);
  else ocur = MFMA32(v0, __builtin_bit_cast(bf16x8, p0), oprev);
  SB();
  EXP_PK(d, sc, 12); SB();
  p1 = u32x4{c[0], c[1], d[0], d[1]};
}
template <class H>
DI void att_block(const KVRegs& kv, char LAS* smem, int lane, int sout, int sin, int bnext, bf16x8& q1, s16x4& q2,
                  f32x16& sa, f32x16& sb, u32x4& p1, f32x16& oprev, f32x16& ocur, H hook) {
  LAS f32x4* so = (LAS f32x4*)(smem + L_RING + sout * 3072) + lane;
  LAS f32x4* si = (LAS f32x4*)(smem + L_RING + sin * 3072) + lane;
  att_step<true, -1>(sa, sb, kv.k1[1], kv.k2[1], q1, q2, kv.v[0][0], kv.v[7][1], p1, oprev, ocur);
  hook(0); SB();
  att_step<false, 1>(sb, sa, kv.k1[2], kv.k2[2], q1, q2, kv.v[1][0], kv.v[0][1], p1, ocur, ocur);
  so[0] = f32x4{oprev[0], oprev[1], oprev[2], oprev[3]};
  hook(1); SB();
  att_step<false, -1>(sa, sb, kv.k1[3], kv.k2[3], q1, q2, kv.v[2][0], kv.v[1][1], p1, ocur, ocur);
  so[64] = f32x4{oprev[4], oprev[5], oprev[6], oprev[7]};
  hook(2); SB();
  att_step<false, 0>(sb, sa, kv.k1[4], kv.k2[4], q1, q2, kv.v[3][0], kv.v[2][1], p1, ocur, ocur);
  so[128] = f32x4{oprev[8], oprev[9], oprev[10], oprev[11]};
  hook(3); SB();
  att_step<false, -1>(sa, sb, kv.k1[5], kv.k2[5], q1, q2, kv.v[4][0], kv.v[3][1], p1, ocur, ocur);
  hook(4);
  bar_lds();
  const bf16x8 q1n = *(const LAS bf16x8*)(smem + L_Q + bnext * 1536 + lane * 16);
  const s16x4 q2n = *(const LAS s16x4*)(smem + L_Q + bnext * 1536 + 1024 + lane * 8);
  SB();
  att_step<false, 3>(sb, sa, kv.k1[6], kv.k2[6], q1, q2, kv.v[5][0], kv.v[4][1], p1, ocur, ocur);
  {
    const f32x4 i0 = si[0], i1 = si[64], i2 = si[128];
#pragma unroll
    for (int e = 0; e < 4; ++e) { oprev[e] = i0[e]; oprev[4 + e] = i1[e]; oprev[8 + e] = i2[e]; }
  }
  hook(5); SB();
  att_step<false, -1>(sa, sb, kv.k1[7], kv.k2[7], q1, q2, kv.v[6][0], kv.v[5][1], p1, ocur, ocur);
  hook(6); SB();
  att_step<false, 2>(sb, sa, kv.k1[0], kv.k2[0], q1n, q2n, kv.v[7][0], kv.v[6][1], p1, ocur, ocur);
  q1 = q1n; q2 = q2n;
}
DI void dma_hook(const DmaJob& dj, unsigned waitmode, int i) {
  if (i == 4) {
    asm volatile("s_cmp_eq_u32 %0, 0\n\ts_cbranch_scc1 1f\n\ts_waitcnt vmcnt(3)\n\ts_cmp_eq_u32 %0, 1\n\ts_cbranch_scc1 1f\n\t"
                 "s_waitcnt vmcnt(0)\n1:" :: "s"(__builtin_amdgcn_readfirstlane(waitmode)) : "memory", "scc");
  } else if (i != 1) {
    const int c = i == 0 ? 0 : (i < 4 ? i - 1 : i - 2);
    dma16_if(dj.on, dj.sb + (size_t)c * dj.sstr, dj.voff[c], dj.lds + c * 1024);
  }
}
DI void poll_words(rsrc_t rs, int voff, int soff, unsigned tag) {
#pragma unroll 1
  for (unsigned spin = 0; spin < SPIN_CAP; ++spin) {
    CFENCE();
    const unsigned v = ld4(rs, voff, soff);
    if (__all(v == tag)) break;
    __builtin_amdgcn_s_sleep(1);
  }
  CFENCE();
}

__global__ __launch_bounds__(512, 2) void fused(const u16* __restrict__ x, const u16* __restrict__ wq,
                                                const u16* __restrict__ wk, const u16* __restrict__ wv,
                                                const u16* __restrict__ wo, const u16* __restrict__ wr,
                                                const u16* __restrict__ wg, const u16* __restrict__ wu,
                                                const u16* __restrict__ wd, char* ws, u16* __restrict__ out) {
  __shared__ __attribute__((aligned(16))) char smem_[LDS_BYTES];
  char LAS* smem = (char LAS*)smem_;
  const unsigned lds0 = (unsigned)(size_t)smem;
  const int tid = threadIdx.x, lane = tid & 63;
  const int w = __builtin_amdgcn_readfirstlane(tid >> 6);
  {
    const char* pc = (const char*)__builtin_amdgcn_s_getpc();
    touch4(pc + w * 4352, (lane < 34 ? lane : 33) * 128, lds0 + L_SCR + 512);
  }
  const int bid = blockIdx.x, xcd = bid & 7, jj = bid >> 3;
  const int qb = xcd * 4 + (jj >> 3), hg = jj & 7, pid = qb * 8 + hg;
  const bool has_ffn = qb < 27;
  const int fcol = pid * 8;
  const int tok0 = pid * 8, tile = tok0 >> 5;
  const rsrc_t rs = __builtin_amdgcn_make_buffer_rsrc(ws, 0, WS_BYTES, 0x00020000);
  LAS float* scr = (LAS float*)(smem + L_SCR);
  LAS unsigned* cnt = (LAS unsigned*)(smem + L_SCR + 96);

  unsigned tag = 0;
  u32x4 late = {0, 0, 0, 0};
  const int mt = w & 3, kh = w >> 2;
  bf16x8 af[10];
  const u16* xpiece = x + (size_t)(qb * 64 + mt * 16) * D_ + kh * 320;
  if (w < 4) {
    const int half = lane >> 5, l32 = lane & 31, tl = 2 * w + half;
    u32x2 xv[5], wv5[5];
    u32x4 xp[10];
#pragma unroll
    for (int i = 0; i < 5; ++i) xv[i] = *(const u32x2*)(x + (size_t)(tok0 + tl) * D_ + i * 128 + l32 * 4);
    const unsigned ep = ld4(rs, WS_EP);
#pragma unroll
    for (int i = 0; i < 5; ++i) wv5[i] = *(const u32x2*)(wr + i * 128 + l32 * 4);
    if (tid < 2) *((LAS u32x4*)cnt + tid) = u32x4{0, 0, 0, 0};
    bar_lds();
    load_piece(xpiece, lane, xp);
    SB();
    float s = 0.f;
#pragma unroll
    for (int i = 0; i < 5; ++i) { s = dot2(xv[i][0], 0x3f803f80u, s); s = dot2(xv[i][1], 0x3f803f80u, s); }

    const float mean = sum32(s) * (1.0f / D_);
    f32x2 v2 = {0.f, 0.f};
#pragma unroll
    for (int i = 0; i < 10; ++i) {
      const f32x2 d = f32x2{bflo(xv[i >> 1][i & 1]), bfhi(xv[i >> 1][i & 1])} - mean;
      v2 += d * d;
    }
    const float sd = ln_sd(sum32(v2[0] + v2[1]) * (1.0f / D_));
    const float mb = rbf(mean), rc = 1.0f / sd;
#pragma unroll
    for (int i = 0; i < 5; ++i) {
      xv[i] = u32x2{ln_pair(xv[i][0], mb, sd, rc), ln_pair(xv[i][1], mb, sd, rc)};
      *(LAS u32x2*)(smem + L_X + tl * 1280 + (((i * 16 + (l32 >> 1)) ^ tl) * 16) + (l32 & 1) * 8) = xv[i];
    }
    if (lane == 0) lds_add(cnt + 0, 1);
    float racc = 0.f;
#pragma unroll
    for (int i = 0; i < 5; ++i) { racc = dot2(xv[i][0], wv5[i][0], racc); racc = dot2(xv[i][1], wv5[i][1], racc); }
    racc = sum32(racc);
    if (l32 == 0) scr[tl] = racc;
    tag = __builtin_amdgcn_readfirstlane(ep) + 1u;
    lds_wait(cnt + 0, 6);

    const int g = lane >> 4, c = lane & 15, q = c >> 2, pp = lane & 3, r8 = c & 7;
    const int mat = w >> 1, nt = w & 1;
    const char LAS* wb = smem + L_WK + mat * 25600 + (8 * g + q) * 40 + nt * 32 + 8 * pp;
    const char LAS* xb0 = smem + L_X + r8 * 1280 + (g ^ r8) * 16;
    const char LAS* xb1 = smem + L_X + r8 * 1280 + ((4 + g) ^ r8) * 16;
    bf16x8 wf[20], xf[20];
#pragma unroll
    for (int s2 = 0; s2 < 20; ++s2) {
      xf[s2] = *(const LAS bf16x8*)(((s2 & 1) ? xb1 : xb0) + (s2 >> 1) * 128);
      wf[s2] = cat8(trread(wb + s2 * 1280), trread(wb + s2 * 1280 + 160));
    }
    f32x4 a0 = {0, 0, 0, 0}, a1 = {0, 0, 0, 0};
    if (mat == 0) {
#pragma unroll
      for (int s2 = 0; s2 < 20; s2 += 2) { a0 = MFMA16(wf[s2], xf[s2], a0); a1 = MFMA16(wf[s2 + 1], xf[s2 + 1], a1); }
    } else {
#pragma unroll
      for (int s2 = 0; s2 < 20; s2 += 2) { a0 = MFMA16(xf[s2], wf[s2], a0); a1 = MFMA16(xf[s2 + 1], wf[s2 + 1], a1); }
    }
    if (lane == 0) lds_add(cnt + 2, 1);
    a0 += a1;
    u32x2 pk = {pk2(a0[0], a0[1]), pk2(a0[2], a0[3])};
    int soff; bool son;
    if (mat == 0) {
      float n2 = dot2(pk[0], pk[0], dot2(pk[1], pk[1], 0.f));
      const int r = (tok0 & 31) + c;
      if (nt == 0) {
        soff = WS_KF1 + tile * 1024 + (r + 32 * (g >> 1)) * 16 + (g & 1) * 8; son = c < 8;
        n2 = sum_halves(sum_rows(n2));
      } else {
        if (lane == 8) pk = u32x2{0x3f803f80u, 0u};
        soff = WS_KF2 + tile * 272 + (lane < 8 ? r : 32) * 8; son = lane <= 8;
      }
      if (lane < 8) scr[8 + nt * 8 + lane] = n2;
    } else {
      const int d = 16 * nt + c, u = (tok0 & 31) >> 3;
      if (d == 20) pk = u32x2{0x3f803f80u, 0x3f803f80u};
      if (d == 21) pk = u32x2{0u, 0u};
      soff = WS_VF + ((tile * 2 + (u >> 1)) * 2 + g) * 352 + d * 16 + (u & 1) * 8; son = g < 2 && d < 22;
    }
    if (son) st8(rs, pk, soff);
    lds_wait(cnt + 2, 4);
    bounce_piece(smem + L_WK + w * 5376, lane, xp, af);
    drain();
    if (lane == 0 && lds_add_rtn(cnt + 3, 1) == 3) {
      f32x4 sv[6];
#pragma unroll
      for (int i = 0; i < 6; ++i) sv[i] = *(const LAS f32x4*)(scr + 4 * i);
      float rm = sv[0][0], km = sv[2][0] + sv[4][0]; int ri = 0;
#pragma unroll
      for (int i = 1; i < 8; ++i) {
        const float rr = sv[i >> 2][i & 3];
        const bool t = rr > rm;
        rm = t ? rr : rm; ri = t ? i : ri;
        km = fmaxf(km, sv[2 + (i >> 2)][i & 3] + sv[4 + (i >> 2)][i & 3]);
      }
      const unsigned kb = (__float_as_uint(km) + 0xffffu) & 0xffff0000u;
      st16(rs, u32x4{tag, __float_as_uint(rm), tag, kb | (unsigned)(tok0 + ri)}, WS_FB + pid * 128);
    }

    lds_wait(cnt + 4, 4);
    {
      unsigned voff[5];
      w_voff(lane, voff);
#pragma unroll
      for (int c = 0; c < 5; ++c)
        dma16((const char*)wq + hg * 160 + (size_t)w * 40960, voff[c], lds0 + L_W + w * 5120 + c * 1024);
    }
  } else {
    u32x4 xp[10];
    unsigned voff[5];
    if (w < 6) {
      u32x4 a[25];
      const char* src = (const char*)(w == 4 ? wk : wv) + lane * 16;
      const int rot = ((jj + xcd * 3) % 5) * 5;
#pragma unroll
      for (int i = 0; i < 25; ++i) {
        a[i] = *(const u32x4*)(src + (i + rot >= 25 ? i + rot - 25 : i + rot) * 1024);
        if (i == 0) bar_lds();
      }
      w_voff(lane, voff);
      SB();
      char LAS* dst = smem + (w == 4 ? L_WK : L_WV) + lane * 16;
#pragma unroll
      for (int i = 0; i < 25; ++i) *(LAS u32x4*)(dst + (i + rot >= 25 ? i + rot - 25 : i + rot) * 1024) = a[i];
      if (lane == 0) lds_add(cnt + 0, 1);
    } else {
      bar_lds();
      w_voff(lane, voff);
      int G = ((jj >> 3) * 4 + (xcd >> 1) + (w - 6) * 8) % 15;
#pragma unroll 1
      for (int i = w - 6; i < 8; ++i) {
        const char* sb = (const char*)wq + hg * 160 + (size_t)(5 + G) * 40960;
#pragma unroll
        for (int c = 0; c < 5; ++c) dma16_if(G * 5 + c != 74, sb, voff[c], lds0 + L_W + (5 + G) * 5120 + c * 1024);
        G = G == 14 ? 0 : G + 1;
      }
    }

    load_piece(xpiece, lane, xp);
    late = *(const u32x4*)((const char*)wq + hg * 160 + 19 * 40960 + voff[4]);
    bounce_piece(smem + L_W + (w - 4) * 5376, lane, xp, af);
#pragma unroll
    for (int s = 0; s < 10; ++s) asm volatile("" : "+v"(af[s]));
    asm volatile("" : "+v"(late));
    if (lane == 0) lds_add(cnt + 4, 1);

    if (w == 4) {
      lds_wait(cnt + 4, 4);
#pragma unroll
      for (int c = 0; c < 5; ++c) dma16((const char*)wq + hg * 160 + 4 * 40960, voff[c], lds0 + L_W + 4 * 5120 + c * 1024);
    }

  }
  float s1 = 0.f, s2q = 0.f;
#pragma unroll
  for (int s = 0; s < 10; ++s) {
    const u32x4 u = __builtin_bit_cast(u32x4, af[s]);
#pragma unroll
    for (int e = 0; e < 4; ++e) { s1 = dot2(u[e], 0x3f803f80u, s1); s2q = dot2(u[e], u[e], s2q); }
  }
  s1 = sum_halves(sum_rows(s1)); s2q = sum_halves(sum_rows(s2q));
  bar_all();

  if (lane < 16) *(LAS f32x2*)(smem + L_STAT + (kh * 64 + mt * 16 + lane) * 8) = f32x2{s1, s2q};
  KVRegs kv;
  const int k2o = (lane < 32 ? lane : 32) * 8, vo = (lane >> 5) * 352 + ((lane & 31) < 21 ? (lane & 31) : 21) * 16;
  auto load_tile = [&](int t) {
    kv.k1[t] = __builtin_bit_cast(bf16x8, ld16(rs, lane * 16, WS_KF1 + (w * 8 + t) * 1024));
    kv.k2[t] = __builtin_bit_cast(s16x4, ld8(rs, k2o, WS_KF2 + (w * 8 + t) * 272));
    kv.v[t][0] = __builtin_bit_cast(bf16x8, ld16(rs, vo, WS_VF + (w * 8 + t) * 1408));
    if (t < 7) kv.v[t][1] = __builtin_bit_cast(bf16x8, ld16(rs, vo + 704, WS_VF + (w * 8 + t) * 1408));
  };
  u32x4 gv[4];
  if (w == 0) {
#pragma unroll
    for (int i = 0; i < 4; ++i)
      asm volatile("global_load_dwordx4 %0, %1, %2 sc1" : "=v"(gv[i]) : "v"(lane * 128 + i * 8192), "s"(ws + WS_FB) : "memory");
  }
  bar_lds();

  if (kh == 1) *(LAS u32x4*)(smem + L_W + 99 * 1024 + lane * 16) = late;
  {
    f32x4 acc[5] = {{0, 0, 0, 0}, {0, 0, 0, 0}, {0, 0, 0, 0}, {0, 0, 0, 0}, {0, 0, 0, 0}};
    f32x4 cs[5] = {{0, 0, 0, 0}, {0, 0, 0, 0}, {0, 0, 0, 0}, {0, 0, 0, 0}, {0, 0, 0, 0}};
    const bf16x8 ones = __builtin_bit_cast(bf16x8, u32x4{0x3f803f80u, 0x3f803f80u, 0x3f803f80u, 0x3f803f80u});
    gemm_half(af, smem + L_W + kh * 51200, lane, acc, [&](int t, const bf16x8 (&wf)[5]) {
      if ((t & 3) == mt) {
#pragma unroll
        for (int i = 0; i < 5; ++i) cs[i] = MFMA16(wf[i], ones, cs[i]);
      }
    });
    write_part(smem + L_PART, kh, mt, lane, acc);
    if ((lane & 15) == 0) {
#pragma unroll
      for (int i = 0; i < 5; ++i) *(LAS f32x4*)(smem + L_CS + w * 320 + i * 64 + (lane >> 4) * 16) = cs[i];
    }
  }
  float mb = 0.f, sc = 0.f;
  if (kh == 0) {
    const f32x2 st = *(const LAS f32x2*)(smem + L_STAT + lane * 8) + *(const LAS f32x2*)(smem + L_STAT + 512 + lane * 8);
    const float mean = st[0] * (1.0f / D_);
    const float sd = ln_sd(fmaxf(fmaf(-mean, mean, st[1] * (1.0f / D_)), 0.f));
    mb = rbf(mean); sc = QSCALE / sd;
    if (w == 1) *(LAS unsigned*)(smem + L_STP + lane * 4) = pk2(mb, sd);
  }
  if (w == 0) {
    asm volatile("s_waitcnt vmcnt(0)" : "+v"(gv[0]), "+v"(gv[1]), "+v"(gv[2]), "+v"(gv[3]) :: "memory");
#pragma unroll 1
      for (unsigned spin = 0; spin < SPIN_CAP; ++spin) {
        bool ok = true;
#pragma unroll
        for (int i = 0; i < 4; ++i) ok &= (gv[i][0] == tag) & (gv[i][2] == tag);
        if (__all(ok)) break;
        if (spin) __builtin_amdgcn_s_sleep(POLL_SLEEP);
        CFENCE();
#pragma unroll
        for (int i = 0; i < 4; ++i)
          if ((gv[i][0] != tag) | (gv[i][2] != tag)) gv[i] = ld16(rs, lane * 128 + i * 8192, WS_FB);
      }
      CFENCE();
      float rm = __uint_as_float(gv[0][1]), kn = __uint_as_float(gv[0][3] & 0xffff0000u);
#pragma unroll
      for (int i = 1; i < 4; ++i) { rm = fmaxf(rm, __uint_as_float(gv[i][1])); kn = fmaxf(kn, __uint_as_float(gv[i][3] & 0xffff0000u)); }
      rm = max64(rm); kn = max64(kn);
      float neg = -4096.f;
#pragma unroll
      for (int i = 0; i < 4; ++i) neg = __uint_as_float(gv[i][1]) == rm ? fmaxf(neg, -(float)(gv[i][3] & 0xffffu)) : neg;
      neg = max64(neg);
      if (lane == 0) {
        *(LAS u32x4*)(smem + L_SUM) = u32x4{__float_as_uint(rm), (unsigned)(int)(-neg) & (S_ - 1), __float_as_uint(kn), tag};
        if (bid == 0) st4(rs, tag, WS_EP);
      }
    }
  bar_lds();

  load_tile(0);
  kv.v[7][1] = __builtin_bit_cast(bf16x8, ld16(rs, vo + 704, WS_VF + (w * 8 + 7) * 1408));
  load_tile(1);
  SB();
  const u32x4 sumv = *(const LAS u32x4*)(smem + L_SUM);
  const float rmax = __uint_as_float(__builtin_amdgcn_readfirstlane(sumv[0]));
  const int ridx = __builtin_amdgcn_readfirstlane(sumv[1]);
  tag = __builtin_amdgcn_readfirstlane(sumv[3]);
  unsigned xs[5] = {0, 0, 0, 0, 0};
  float qn2 = 0.f;
  if (tid < 256) {
    const int b = tid >> 5, cc = tid & 31, tok = lane;
    const char LAS* pp = smem + L_PART + tok * 336 + w * 80;
    f32x4 cq = *(const LAS f32x4*)(smem + L_CS + (lane & 7) * 320 + w * 80 + (lane < 40 ? lane >> 3 : 0) * 16);
#pragma unroll
    for (int e = 0; e < 4; ++e) { cq[e] += dpp_mov<0xB1>(cq[e]); cq[e] += dpp_mov<0x4E>(cq[e]); cq[e] += dpp_mov<0x141>(cq[e]); }
    unsigned qv[10];
#pragma unroll
    for (int j = 0; j < 5; ++j) {
      f32x4 a = *(const LAS f32x4*)(pp + j * 16) + *(const LAS f32x4*)(pp + 21504 + j * 16);
#pragma unroll
      for (int e = 0; e < 4; ++e) a[e] = fmaf(-mb, rdlane(cq[e], j * 8), a[e]) * sc;
      qv[2 * j] = pk2(a[0], a[1]); qv[2 * j + 1] = pk2(a[2], a[3]);
      qn2 = dot2(qv[2 * j], qv[2 * j], qn2); qn2 = dot2(qv[2 * j + 1], qv[2 * j + 1], qn2);
    }
    char LAS* qd = smem + L_Q + b * 1536;
    *(LAS u32x4*)(qd + cc * 16) = u32x4{qv[0], qv[1], qv[2], qv[3]};
    *(LAS u32x4*)(qd + (cc + 32) * 16) = u32x4{qv[4], qv[5], qv[6], qv[7]};
    *(LAS u32x2*)(qd + 1024 + cc * 8) = u32x2{qv[8], qv[9]};
    const float m = __builtin_amdgcn_sqrtf(qn2 * __uint_as_float(sumv[2])) * 1.01f;
    *(LAS u32x2*)(qd + 1024 + (cc + 32) * 8) = u32x2{pk2(-m, 0.f), 0u};
  } else {
#pragma unroll
    for (int i = 0; i < 12; ++i) *(LAS f32x4*)(smem + L_RING + i * 4096 + (tid - 256) * 16) = f32x4{0, 0, 0, 0};
  }
  bar_lds();

  load_tile(2);
  load_tile(3);
  SB();
  const unsigned stp0 = *(const LAS unsigned*)(smem + L_STP + (tid / 10) * 4);
  const unsigned stp1 = *(const LAS unsigned*)(smem + L_STP + (tid < 128 ? (512 + tid) / 10 : 63) * 4);

  const f32x16 zero16 = {0, 0, 0, 0, 0, 0, 0, 0, 0, 0, 0, 0, 0, 0, 0, 0};
  f32x16 oa = zero16, ob = zero16;
  bf16x8 q1 = *(const LAS bf16x8*)(smem + L_Q + w * 1536 + lane * 16);
  s16x4 q2 = *(const LAS s16x4*)(smem + L_Q + w * 1536 + 1024 + lane * 8);
  f32x16 sa = MFMA32(kv.k1[0], q1, zero16), sb;
  sa = MFMA32K8(kv.k2[0], q2, sa);
  u32x4 p1 = {0, 0, 0, 0};
  att_block(kv, smem, lane, ((w + 7) & 7) + 8, ((w + 1) & 7) + 8, (w + 1) & 7, q1, q2, sa, sb, p1, ob, oa,
            [&](int i) {
              if (i < 4) load_tile(4 + i);
              if (i == 2 && w == 6) {
#pragma unroll
                for (int k = 0; k < 5; ++k)
                  asm volatile("global_load_dword %0, %1, %2 offset:%3" : "=v"(xs[k]) : "v"(lane * 4), "s"(x + (size_t)ridx * D_), "i"(k * 256) : "memory");
              }
            });
  if (w == 6) {
    asm volatile("s_waitcnt vmcnt(0)" : "+v"(xs[0]), "+v"(xs[1]), "+v"(xs[2]), "+v"(xs[3]), "+v"(xs[4]) :: "memory");
    float s = 0.f;
#pragma unroll
    for (int i = 0; i < 5; ++i) s = dot2(xs[i], 0x3f803f80u, s);
    const float mean = sum_halves(sum32(s)) * (1.0f / D_);
    f32x2 v2 = {0.f, 0.f};
#pragma unroll
    for (int i = 0; i < 5; ++i) { const f32x2 d = f32x2{bflo(xs[i]), bfhi(xs[i])} - mean; v2 += d * d; }
    const float sd = ln_sd(sum_halves(sum32(v2[0] + v2[1])) * (1.0f / D_));
    const float mb = rbf(mean), rc = 1.0f / sd;
#pragma unroll
    for (int i = 0; i < 5; ++i) *(LAS unsigned*)(smem + L_XS + i * 256 + lane * 4) = ln_pair(xs[i], mb, sd, rc);
  }
  DmaJob dj;
  if (w < 6) {
    const int ln = opaque(lane);
    const unsigned vo2 = w < 4 ? ln * (F_ * 2) : ln * 16;
#pragma unroll
    for (int i = 0; i < 5; ++i) dj.voff[i] = vo2;
    dj.sstr = w < 4 ? 64 * F_ * 2 : 1024;
    dj.sb = (w < 4 ? (const char*)((w < 2 ? wg : wu) + fcol) : (const char*)(wd + (size_t)fcol * D_)) + (size_t)(w & 1) * 5 * dj.sstr;
    dj.lds = lds0 + L_G + w * 5120;
    dj.on = has_ffn;
  } else {
    const int ln = opaque(lane);
    const char* qbase = (const char*)wo + hg * 160 + (size_t)(jj >> 3) * 204800 + (size_t)(w - 6) * 81920;
    touch4(qbase, (ln >> 1) * 1280 + (ln & 1) * 128, lds0 + L_FRED + 512);
    touch4(qbase + 40960, (ln >> 1) * 1280 + (ln & 1) * 128, lds0 + L_FRED + 512);
    if (w == 7) touch4(qbase + 81920, (ln >> 1) * 1280 + (ln & 1) * 128, lds0 + L_FRED + 512);
#pragma unroll
    for (int i = 0; i < 5; ++i) dj.voff[i] = 0;
    dj.sstr = 0; dj.sb = (const char*)wo; dj.lds = lds0; dj.on = 0;
  }
  u32x4 xe[2], ge[2];
  f32x4 yl[3];
  const bool sel_blk = (ridx >> 6) == qb;
#pragma unroll 1
  for (int attempt = 0;; ++attempt) {
#pragma unroll 1
    for (int st = 0; st < 8; st += 2) {
      int flag_off = -1;
      if (attempt == 0) {
        if (st == 4) {
          if (has_ffn && w < 4) {
            const int ln = opaque(lane);
            const int g = ln >> 4, q = (ln & 15) >> 2, pp = ln & 3;
            const char LAS* gb = smem + L_G + (pp >> 1) * 10240 + (8 * g + q) * 16 + (pp & 1) * 8 + w * 2560;
            const char LAS* xsp = smem + L_XS + w * 320 + g * 16;
            f32x4 a = {0, 0, 0, 0};
#pragma unroll
            for (int s = 0; s < 5; ++s)
              a = MFMA16(*(const LAS bf16x8*)(xsp + s * 64), cat8(trread(gb + s * 512), trread(gb + s * 512 + 64)), a);
            if (ln < 16) *(LAS float*)(smem + L_FRED + ln * 16 + w * 4) = a[0];
          }
        } else if (st == 6) {
          drain();
          if (has_ffn && w == 2) flag_off = WS_FY + pid * 128;
        }
      }
      if (attempt != 0 || st != 0)
        att_block(kv, smem, lane, ((w + st + 7) & 7) + 8, ((w + st + 1) & 7) + 8, (w + st + 1) & 7, q1, q2, sa, sb, p1, ob, oa,
                  [&](int i) { if (i == 5 && flag_off >= 0 && lane == 0) st4(rs, tag, flag_off); });
      unsigned waitmode = 0, yflag = 0;
      bool ylook = false;
      if (attempt == 0) {
        if (st == 2) {
          waitmode = 1;
          w_voff(opaque(lane), dj.voff);
          dj.sstr = 0;
          dj.sb = (const char*)wo + hg * 160 + (size_t)w * 40960; dj.lds = lds0 + L_W + w * 5120; dj.on = 1;
        } else if (st == 4) {
          dj.sb = (const char*)wo + hg * 160 + (size_t)(w < 2 ? 8 + w : 15 + w) * 40960;
          dj.lds = lds0 + (w < 2 ? L_W + (8 + w) * 5120 : L_WHI + (5 + w) * 5120); dj.on = w < 4;
          if (has_ffn && w >= 4) {
            const int ln = opaque(lane);
            const int cl = ln < 40 ? ln : 39;
            const f32x4 g4 = *(const LAS f32x4*)(smem + L_FRED + (ln & 15) * 16);
            u32x2 wv2[8];
#pragma unroll
            for (int c = 0; c < 8; ++c) wv2[c] = *(const LAS u32x2*)(smem + L_DN + c * 1280 + (w - 4) * 320 + cl * 8);
            const float gu = rbf((g4[0] + g4[1]) + (g4[2] + g4[3]));
            const float up = dpp_mov<0x128>(gu);
            const float hval = rbf(rbf(silu_f(gu)) * up);
            f32x4 y = {0, 0, 0, 0};
#pragma unroll
            for (int c = 0; c < 8; ++c) {
              const float hc = rdlane(hval, c);
              y[0] = fmaf(hc, bflo(wv2[c][0]), y[0]); y[1] = fmaf(hc, bfhi(wv2[c][0]), y[1]);
              y[2] = fmaf(hc, bflo(wv2[c][1]), y[2]); y[3] = fmaf(hc, bfhi(wv2[c][1]), y[3]);
            }
            if (ln < 40) st16(rs, __builtin_bit_cast(u32x4, y), ln * 16 + (ln >= 20 ? 2560 - 320 : 0),
                              WS_YP + (qb * 8 + (w - 4) * 2) * 2560 + hg * 320);
          }
        } else if (st == 6) {
          dj.sb = (const char*)wo + hg * 160 + (size_t)(w < 7 ? 10 + w : 19) * 40960;
          dj.lds = lds0 + L_WHI + (w < 7 ? w : 9) * 5120; dj.on = 1;
          if (has_ffn && w == 3) {
            asm volatile("global_load_dword %0, %1, %2 sc1" : "=v"(yflag) : "v"((lane & 7) * 128), "s"(ws + WS_FY + qb * 1024) : "memory");
            ylook = true;
          }
        }
      }
      att_block(kv, smem, lane, (w + st) & 7, (w + st + (st == 6 ? 1 : 2)) & 7, (w + st + 2) & 7, q1, q2, sa, sb, p1, oa, ob,
                [&](int i) {
                  dma_hook(dj, waitmode, i);
                  if (i == 3 && ylook) {
                    asm volatile("s_waitcnt vmcnt(0)" : "+v"(yflag) :: "memory");
                    if (!__all(yflag == tag)) poll_words(rs, (lane & 7) * 128, WS_FY + qb * 1024, tag);
                    CFENCE();
                    const int ln = opaque(lane);
#pragma unroll
                    for (int k = 0; k < 3; ++k)
                      yl[k] = __builtin_bit_cast(f32x4, ld16(rs, (ln < 60 && k * 60 + ln < 160) ? k * 960 + ln * 16 : 0x7ff00000, WS_YP + pid * 2560));
                  }
                });
      dj.on = 0;

    }
    __builtin_amdgcn_s_setprio(0);
    ob = MFMA32(kv.v[7][1], __builtin_bit_cast(bf16x8, p1), ob);
#pragma unroll
    for (int e = 0; e < 12; ++e) ob[e] += oa[e];

    const int tid4 = opaque(tid), ln4 = tid4 & 63;
    const float l = hi_half(ob[8]);
    const bool bad = attempt == 0 && __any(!(l >= 5.4e-20f && l <= 1e30f));
    LAS unsigned* ocnt = (LAS unsigned*)(smem + L_CNT);
    if (!bad) {
      const int b = (w + 7) & 7, hh = b >> 1, tok = qb * 64 + (b & 1) * 32 + (ln4 & 31), h4 = ln4 >> 5;
      const float inv = 1.0f / l;
      unsigned A0 = pk2(ob[0] * inv, ob[1] * inv), A1 = pk2(ob[2] * inv, ob[3] * inv);
      unsigned B0 = pk2(ob[4] * inv, ob[5] * inv), B1 = pk2(ob[6] * inv, ob[7] * inv);
      unsigned C0 = pk2(ob[8] * inv, ob[9] * inv), C1 = pk2(ob[10] * inv, ob[11] * inv);
      const int base = WS_O + ((tok >> 4) * 80 + hg * 10 + ((hh * 5) >> 1)) * 256 + (tok & 15) * 16;
      if ((hh & 1) == 0) {
        auto s0 = __builtin_amdgcn_permlane32_swap(A0, B0, false, false);
        auto s1 = __builtin_amdgcn_permlane32_swap(A1, B1, false, false);
        st16(rs, u32x4{s0[0], s1[0], s0[1], s1[1]}, base + h4 * 256);
        if (h4 == 0) st8(rs, u32x2{C0, C1}, base + 512);
      } else {
        auto s0 = __builtin_amdgcn_permlane32_swap(A0, C0, false, false);
        auto s1 = __builtin_amdgcn_permlane32_swap(A1, C1, false, false);
        st16(rs, h4 == 0 ? u32x4{s0[1], s1[1], B0, B1} : u32x4{B0, B1, s0[0], s1[0]}, base + 256 + h4 * 256);
        if (h4 == 0) st8(rs, u32x2{s0[0], s1[0]}, base + 8);
      }
    }
    drain();
    if (ln4 == 0 && lds_add_rtn(ocnt, bad ? 0x101u : 1u) == 7u && !bad) st4(rs, tag, WS_FC + pid * 128);
    if (attempt == 0 && has_ffn && w == 3) {
      f32x4 a = (yl[0] + yl[1]) + yl[2];
#pragma unroll
      for (int e = 0; e < 4; ++e) a[e] = (a[e] + __shfl_down(a[e], 20)) + __shfl_down(a[e], 40);
      if (ln4 < 20) st16(rs, __builtin_bit_cast(u32x4, a), ln4 * 16, WS_YQ + (hg * 27 + qb) * 320);
      drain();
      if (ln4 == 0) st4(rs, tag, WS_FZ + hg * 4096 + qb * 128);
    }

#pragma unroll
    for (int i = 0; i < 2; ++i) {
      if (i == 1 && w >= 2) { xe[1] = u32x4{0, 0, 0, 0}; break; }
      const int n = i * 512 + tid4;
      const int row = n / 10, ch = n - row * 10, tok = qb * 64 + row;
      xe[i] = *(const u32x4*)(x + (size_t)tok * D_ + hg * 80 + ch * 8);
    }
    const bool selrow0 = qb * 64 + tid4 / 10 == ridx, selrow1 = qb * 64 + (512 + tid4) / 10 == ridx;
#pragma unroll
    for (int i = 0; i < 2; ++i) {
      if (i == 1 && w >= 2) { ge[1] = u32x4{0, 0, 0, 0}; break; }
      const unsigned stp = i == 0 ? stp0 : stp1;
      const float mb = bflo(stp), sd = bfhi(stp), rc = 1.0f / sd;
#pragma unroll
      for (int e = 0; e < 4; ++e) ge[i][e] = ln_pair(xe[i][e], mb, sd, rc);
      if (!(i == 0 ? selrow0 : selrow1)) {
#pragma unroll
        for (int e = 0; e < 4; ++e) ge[i][e] = pk2(silu_f(bflo(ge[i][e])), silu_f(bfhi(ge[i][e])));
      }
    }
    if (w == 7) {
#pragma unroll 1
      for (unsigned spin = 0; spin < SPIN_CAP; ++spin) {
        CFENCE();
        const unsigned v = ld4(rs, (ln4 & 7) * 128, WS_FC + qb * 1024);
        if (__all(v == tag) || lds_get(ocnt) > 0xffu) break;
        __builtin_amdgcn_s_sleep(1);
      }
    }
    bar_all();
    if (lds_get(ocnt) <= 0xffu) break;
    {
      const int c32 = ln4 & 31, h = ln4 >> 5;
#pragma unroll 1
      for (int b = 0; b < 8; ++b) {
        const bf16x8 fq1 = *(const LAS bf16x8*)(smem + L_Q + b * 1536 + ln4 * 16);
        s16x4 fq2 = *(const LAS s16x4*)(smem + L_Q + b * 1536 + 1024 + ln4 * 8);
        if (h == 1) fq2 = s16x4{0, 0, 0, 0};
        float mx = -INFINITY;
#pragma unroll
        for (int t = 0; t < 8; ++t) {
          f32x16 s = MFMA32(kv.k1[t], fq1, zero16);
          s = MFMA32K8(kv.k2[t], fq2, s);
#pragma unroll
          for (int e = 0; e < 16; ++e) mx = fmaxf(mx, s[e]);
        }
        mx = fmaxf(mx, __shfl_xor(mx, 32));
        if (h == 0) *(LAS float*)(smem + L_MAXS + (w * 256 + b * 32 + c32) * 4) = mx;
      }
      bar_lds();
      if (tid4 < 256) {
        float mx = -INFINITY;
#pragma unroll
        for (int i = 0; i < 8; ++i) mx = fmaxf(mx, *(LAS float*)(smem + L_MAXS + (i * 256 + tid4) * 4));
        const float hi = rbf(mx), lo = mx - hi;
        *(LAS u32x2*)(smem + L_Q + (tid4 >> 5) * 1536 + 1024 + ((tid4 & 31) + 32) * 8) = u32x2{pk2(-hi, -lo), 0u};
      }
      bar_lds();
#pragma unroll
      for (int i = 0; i < 6; ++i) *(LAS f32x4*)(smem + L_RING + i * 8192 + tid4 * 16) = f32x4{0, 0, 0, 0};
      bar_lds();
      q1 = *(const LAS bf16x8*)(smem + L_Q + w * 1536 + lane * 16);
      q2 = *(const LAS s16x4*)(smem + L_Q + w * 1536 + 1024 + lane * 8);
      sa = MFMA32(kv.k1[0], q1, zero16);
      sa = MFMA32K8(kv.k2[0], q2, sa);
      p1 = u32x4{0, 0, 0, 0};
      oa = zero16; ob = zero16;
    }
  }

  const int tid5 = opaque(tid), ln5 = tid5 & 63;
#pragma unroll
  for (int s = 0; s < 5; ++s)
    af[s] = __builtin_bit_cast(bf16x8, ld16(rs, ln5 * 16 + s * 1024, WS_O + ((qb * 4 + mt) * 20 + kh * 10) * 1024));
  LAS float* ys = (LAS float*)(smem + L_YS);
  {
    f32x4 acc[5] = {{0, 0, 0, 0}, {0, 0, 0, 0}, {0, 0, 0, 0}, {0, 0, 0, 0}, {0, 0, 0, 0}};
    gemm_half(af, smem + (kh ? L_WHI : L_W), ln5, acc, [&](int t, const bf16x8 (&)[5]) {
      if (t < 5) af[t + 5] = __builtin_bit_cast(bf16x8, ld16(rs, ln5 * 16 + (t + 5) * 1024, WS_O + ((qb * 4 + mt) * 20 + kh * 10) * 1024));
    });
    write_part(smem + L_PART2, kh, mt, ln5, acc);
  }

  f32x4 yt[9];
  if (sel_blk && w == 0) {
    poll_words(rs, (ln5 < 27 ? ln5 : 26) * 128, WS_FZ + hg * 4096, tag);
#pragma unroll
    for (int i = 0; i < 9; ++i) yt[i] = __builtin_bit_cast(f32x4, ld16(rs, (ln5 < 60 ? ln5 : 59) * 16 + i * 960, WS_YQ + hg * 8640));
  }
  bar_lds();
  auto finish = [&](int i, bool sel_pass) {
    const int n = i * 512 + tid5;
    const int row = n / 10, ch = n - row * 10, tok = qb * 64 + row;
    if (n < 640 && (tok == ridx) == sel_pass) {
      const char LAS* pp = smem + L_PART2 + row * 336 + ch * 32;
      const f32x4 s0 = *(const LAS f32x4*)(pp) + *(const LAS f32x4*)(pp + 21504);
      const f32x4 s1 = *(const LAS f32x4*)(pp + 16) + *(const LAS f32x4*)(pp + 21504 + 16);
      if (sel_pass) {
#pragma unroll
        for (int e = 0; e < 4; ++e)
          ge[i][e] = pk2(silu_f(rbf(bflo(ge[i][e]) + ys[ch * 8 + 2 * e])), silu_f(rbf(bfhi(ge[i][e]) + ys[ch * 8 + 2 * e + 1])));
      }
      u32x4 r;
      r[0] = pk2(bflo(xe[i][0]) + rbf(rbf(s0[0]) * bflo(ge[i][0])), bfhi(xe[i][0]) + rbf(rbf(s0[1]) * bfhi(ge[i][0])));
      r[1] = pk2(bflo(xe[i][1]) + rbf(rbf(s0[2]) * bflo(ge[i][1])), bfhi(xe[i][1]) + rbf(rbf(s0[3]) * bfhi(ge[i][1])));
      r[2] = pk2(bflo(xe[i][2]) + rbf(rbf(s1[0]) * bflo(ge[i][2])), bfhi(xe[i][2]) + rbf(rbf(s1[1]) * bfhi(ge[i][2])));
      r[3] = pk2(bflo(xe[i][3]) + rbf(rbf(s1[2]) * bflo(ge[i][3])), bfhi(xe[i][3]) + rbf(rbf(s1[3]) * bfhi(ge[i][3])));
      *(u32x4*)(out + (size_t)tok * D_ + hg * 80 + ch * 8) = r;
    }
  };
#pragma unroll 1
  for (int pass = 0; pass < 2; ++pass) {
    finish(0, pass == 1);
    finish(1, pass == 1);
    if (!sel_blk || pass == 1) break;
    if (w == 0) {
      f32x4 a = yt[0];
#pragma unroll
      for (int i = 1; i < 9; ++i) a += yt[i];
      const float rsig = rbf(sigm_f(rbf(rmax)));
#pragma unroll
      for (int e = 0; e < 4; ++e) a[e] = rbf(rbf((a[e] + __shfl_down(a[e], 20)) + __shfl_down(a[e], 40)) * rsig);
      if (ln5 < 20) *(LAS f32x4*)(ys + ln5 * 4) = a;
    }
    bar_lds();
  }

}

extern "C" void kernel_launch(void* const* d_in, const int* in_sizes, int n_in, void* d_out, int out_size,
                              void* d_ws, size_t ws_size, hipStream_t stream) {
  fused<<<256, 512, 0, stream>>>((const u16*)d_in[0], (const u16*)d_in[1], (const u16*)d_in[2], (const u16*)d_in[3],
                                 (const u16*)d_in[4], (const u16*)d_in[6], (const u16*)d_in[7], (const u16*)d_in[8],
                                 (const u16*)d_in[9], (char*)d_ws, (u16*)d_out);
}
```
